```python
import torch

def _softmax(x, axis=-1):
    return torch.softmax(x, dim=axis)
def _gelu(x):
    return torch.nn.functional.gelu(x, approximate="tanh")
def _silu(x):
    return torch.nn.functional.silu(x)
def _sigmoid(x):
    return torch.sigmoid(x)
def _layernorm(x, axis=-1, eps=1e-5):
    m = x.mean(dim=axis, keepdim=True)
    v = x.var(dim=axis, keepdim=True, unbiased=False)
    return (x - m) / torch.sqrt(v + eps)
def _scatter_add(tgt, idx, src, axis=0):
    return tgt.clone().index_add_(axis, idx, src)
def _segment_sum(src, seg, n):
    out = torch.zeros((n,) + src.shape[1:], dtype=src.dtype)
    return out.index_add_(0, seg, src)
def _pad(x, pw):
    flat = tuple(v for pair in reversed(pw) for v in pair)
    return torch.nn.functional.pad(x, flat)

kernel_name = 'sequential_preLN__sliding_local_dilated__glu_gated_glu__prompt'

def setup_inputs():

    _w = (lambda a: (lambda t: t.to(torch.bfloat16) if t.is_floating_point() else t)(torch.as_tensor(a)))
    def _A_setup():
        B, S, D = 1, 2048, 576
        return {
            "x": _w((lambda *s: torch.randn(*s, dtype=torch.bfloat16) * 0.05)(B, S, D)),
            "wqkv": _w((lambda *s: torch.randn(*s, dtype=torch.bfloat16) * 0.05)(D, 3 * D)),
            "wo": _w((lambda *s: torch.randn(*s, dtype=torch.bfloat16) * 0.05)(D, D)),
        }
    _A_in = _A_setup()

    def _B_setup():
        B, S, D, F = 1, 2048, 576, 4608
        return {
            "x": _w((lambda *s: torch.randn(*s, dtype=torch.bfloat16) * 0.05)(B, S, D)),
            "w_gate": _w((lambda *s: torch.randn(*s, dtype=torch.bfloat16) * 0.05)(D, F)),
            "w_up": _w((lambda *s: torch.randn(*s, dtype=torch.bfloat16) * 0.05)(D, F)),
            "w_down": _w((lambda *s: torch.randn(*s, dtype=torch.bfloat16) * 0.05)(F, D)),
        }
    _B_in = _B_setup()

    return {
        "A_x": _A_in["x"],
        "A_wqkv": _A_in["wqkv"],
        "A_wo": _A_in["wo"],
        "B_x": _B_in["x"],
        "B_w_gate": _B_in["w_gate"],
        "B_w_up": _B_in["w_up"],
        "B_w_down": _B_in["w_down"],
    }

def reference(A_x, A_wqkv, A_wo, B_x, B_w_gate, B_w_up, B_w_down):
    def _A_ref(x, wqkv, wo):
        B, S, D = x.shape
        H = 32; Dh = D // H
        W, dil = 8, 2
        qkv = (lambda x, *d: x.permute(*d))((x @ wqkv).reshape(B, S, 3, H, Dh), 2, 0, 3, 1, 4)
        q, k, v = qkv[0], qkv[1], qkv[2]
        scale = Dh ** -0.5
        pos = torch.arange(S)
        off = torch.arange(W) * dil
        idx = pos[:, None] - off[None, :]
        mask = idx >= 0
        idx_c = torch.where(mask, idx, 0).reshape(-1)
        kg = k[:, :, idx_c, :].reshape(B, H, S, W, Dh)
        vg = v[:, :, idx_c, :].reshape(B, H, S, W, Dh)
        logits = torch.einsum("bhsd,bhswd->bhsw", q, kg) * scale
        logits = torch.where(mask[None, None, :, :], logits, -1e9)
        attn = _softmax(logits, axis=-1)
        o = torch.einsum("bhsw,bhswd->bhsd", attn, vg)
        return (lambda x, *d: x.permute(*d))(o, 0, 2, 1, 3).reshape(B, S, D) @ wo

    def _B_ref(x, w_gate, w_up, w_down):
        g = torch.einsum("bsd,df->bsf", x, w_gate)
        u = torch.einsum("bsd,df->bsf", x, w_up)
        h = _sigmoid(g) * u
        return torch.einsum("bsf,fd->bsd", h, w_down)

    x = A_x
    h = x + _A_ref(_layernorm(x, axis=-1), A_wqkv, A_wo)
    h2 = h + _B_ref(_layernorm(h, axis=-1), B_w_gate, B_w_up, B_w_down)
    return h2
```

```cpp
#include <hip/hip_runtime.h>
#include <hip/hip_bf16.h>
#include <stdint.h>

typedef unsigned short u16;
__device__ __forceinline__ float bf2f(u16 v) { return __uint_as_float(((unsigned)v) << 16); }
__device__ __forceinline__ u16 f2bf(float f) { __bf16 b = (__bf16)f; return __builtin_bit_cast(u16, b); }
__device__ __forceinline__ float rbf(float f) { return bf2f(f2bf(f)); }

constexpr int S = 2048, D = 576, F = 4608, H = 32, DH = 18;

__global__ void ln_k(const u16* x, u16* y) {
  int row = blockIdx.x; int t = threadIdx.x;
  float s = 0;
  for (int i = t; i < D; i += 64) s += bf2f(x[row * D + i]);
  for (int o = 32; o; o >>= 1) s += __shfl_xor(s, o);
  float m = s / D;
  float v = 0;
  for (int i = t; i < D; i += 64) { float d = bf2f(x[row * D + i]) - m; v += d * d; }
  for (int o = 32; o; o >>= 1) v += __shfl_xor(v, o);
  v /= D;
  float mb = rbf(m), vb = rbf(v);
  float sd = rbf(sqrtf(rbf(vb + 1e-5f)));
  for (int i = t; i < D; i += 64) y[row * D + i] = f2bf(rbf(bf2f(x[row * D + i]) - mb) / sd);
}
__global__ void gemm_k(const u16* A, const u16* B, u16* C, const u16* res, int M, int N, int K) {
  int n = blockIdx.x * 64 + threadIdx.x; int m = blockIdx.y;
  float acc = 0;
  for (int k = 0; k < K; ++k) acc += bf2f(A[m * K + k]) * bf2f(B[(size_t)k * N + n]);
  if (res) C[(size_t)m * N + n] = f2bf(bf2f(res[m * N + n]) + rbf(acc));
  else C[(size_t)m * N + n] = f2bf(acc);
}
__global__ void attn_k(const u16* qkv, u16* o) {
  int t = blockIdx.x; int h = threadIdx.x; if (h >= H) return;
  float scale = rbf(rsqrtf((float)DH));
  float lg[8]; float mx = -1e30f;
  for (int w = 0; w < 8; ++w) {
    int j = t - 2 * w;
    if (j < 0) { lg[w] = -1e9f; }
    else {
      float a = 0;
      for (int d = 0; d < DH; ++d) a += bf2f(qkv[t * 3 * D + h * DH + d]) * bf2f(qkv[j * 3 * D + D + h * DH + d]);
      lg[w] = rbf(rbf(a) * 0.23570226f);
    }
    mx = fmaxf(mx, lg[w]);
  }
  float sum = 0;
  for (int w = 0; w < 8; ++w) { lg[w] = expf(lg[w] - mx); sum += lg[w]; }
  for (int w = 0; w < 8; ++w) lg[w] = rbf(lg[w] / sum);
  for (int d = 0; d < DH; ++d) {
    float a = 0;
    for (int w = 0; w < 8; ++w) { int j = t - 2 * w; if (j < 0) j = 0; a += lg[w] * bf2f(qkv[j * 3 * D + 2 * D + h * DH + d]); }
    o[t * D + h * DH + d] = f2bf(a);
  }
}
__global__ void glu_k(const u16* g, const u16* u, u16* a, int n) {
  int i = blockIdx.x * 256 + threadIdx.x; if (i >= n) return;
  float gv = bf2f(g[i]); float s = rbf(1.f / (1.f + expf(-gv)));
  a[i] = f2bf(s * bf2f(u[i]));
}

extern "C" void kernel_launch(void* const* d_in, const int* in_sizes, int n_in,
                              void* d_out, int out_size, void* d_ws, size_t ws_size,
                              hipStream_t stream) {
  const u16* x = (const u16*)d_in[0];
  const u16* wqkv = (const u16*)d_in[1];
  const u16* wo = (const u16*)d_in[2];
  const u16* wg = (const u16*)d_in[4];
  const u16* wu = (const u16*)d_in[5];
  const u16* wd = (const u16*)d_in[6];
  u16* out = (u16*)d_out;
  u16* ws = (u16*)d_ws;
  u16* xn = ws; u16* qkv = xn + S * D; u16* o = qkv + S * 3 * D; u16* h = o + S * D; u16* hn = h + S * D;
  u16* g = hn + S * D; u16* u = g + (size_t)S * F; u16* a = u + (size_t)S * F;
  ln_k<<<S, 64, 0, stream>>>(x, xn);
  gemm_k<<<dim3(3 * D / 64, S), 64, 0, stream>>>(xn, wqkv, qkv, nullptr, S, 3 * D, D);
  attn_k<<<S, 64, 0, stream>>>(qkv, o);
  gemm_k<<<dim3(D / 64, S), 64, 0, stream>>>(o, wo, h, x, S, D, D);
  ln_k<<<S, 64, 0, stream>>>(h, hn);
  gemm_k<<<dim3(F / 64, S), 64, 0, stream>>>(hn, wg, g, nullptr, S, F, D);
  gemm_k<<<dim3(F / 64, S), 64, 0, stream>>>(hn, wu, u, nullptr, S, F, D);
  glu_k<<<(S * F + 255) / 256, 256, 0, stream>>>(g, u, a, S * F);
  gemm_k<<<dim3(D / 64, S), 64, 0, stream>>>(a, wd, out, h, S, D, F);
}
```

```cpp
#include <hip/hip_runtime.h>
#include <stdint.h>

typedef unsigned short u16;
typedef unsigned long long u64;
typedef __attribute__((ext_vector_type(8))) __bf16 bf16x8;
typedef __attribute__((ext_vector_type(2))) __bf16 bf16x2;
typedef __attribute__((ext_vector_type(4))) short s16x4;
typedef __attribute__((ext_vector_type(4))) float f32x4;
typedef __attribute__((ext_vector_type(2))) float f32x2;
typedef __attribute__((ext_vector_type(16))) float f32x16;
typedef __attribute__((ext_vector_type(4))) unsigned u32x4;
typedef __attribute__((ext_vector_type(2))) unsigned u32x2;
#define DI __device__ __forceinline__
#define LDSP(T, a) ((__attribute__((address_space(3))) T*)(a))
#define AINL __attribute__((always_inline))

template <int I> struct IC { static constexpr int v = I; };
template <int B, int E, class F> DI void sfor(F&& f) {
  if constexpr (B + 1 == E) f(IC<B>{});
  else if constexpr (B < E) { sfor<B, (B + E) / 2>(f); sfor<(B + E) / 2, E>(f); }
}

constexpr int S_ = 2048, D_ = 576, F_ = 4608;

constexpr size_t WS_OF   = 0;
constexpr size_t WS_HC   = WS_OF + (size_t)S_ * D_ * 2;
constexpr size_t WS_HF   = WS_HC + (size_t)S_ * D_ * 2;
constexpr size_t WS_ACT  = WS_HF + (size_t)S_ * D_ * 2;
constexpr size_t WS_PART = WS_ACT + (size_t)S_ * F_ * 2;
constexpr size_t PART_BLK = 28672;
constexpr size_t WS_WDP  = WS_PART + (size_t)256 * PART_BLK;
constexpr size_t WS_SSQ  = WS_WDP + (size_t)16 * 324 * 1024;
constexpr size_t WS_MEANX = WS_SSQ + (size_t)10 * S_ * 4;
constexpr size_t WS_RM   = WS_MEANX + (size_t)S_ * 4;
constexpr size_t WS_STAMP = WS_RM + 4096;

#define STAMP_BEGIN()
#define STAMP_MID(i)
#define STAMP_END(ws, k)
#define STAMP_ARG
#define STAMP_PASS

DI float bf2f(u16 v) { return __uint_as_float(((unsigned)v) << 16); }
DI float rbf(float f) { return (float)(__bf16)f; }
DI unsigned pk2(float a, float b) { bf16x2 v = {(__bf16)a, (__bf16)b}; return __builtin_bit_cast(unsigned, v); }
DI float lo16(unsigned u) { return __uint_as_float(u << 16); }
DI float hi16(unsigned u) { return __uint_as_float(u & 0xffff0000u); }

template <int N> DI void wait_vm() { asm volatile("s_waitcnt vmcnt(%0)" ::"i"(N) : "memory"); }
template <int N> DI void wait_vm_tie(u32x4& a) { asm volatile("s_waitcnt vmcnt(%1)" : "+v"(a) : "i"(N) : "memory"); }
DI void barrier() { __builtin_amdgcn_s_barrier(); }
#define SCHED() __builtin_amdgcn_sched_barrier(0)

DI void glds16(const void* sbase, unsigned voff, unsigned lds) {
  asm volatile("s_mov_b32 m0, %2\n\ts_nop 0\n\tglobal_load_lds_dwordx4 %0, %1" ::"v"(voff), "s"(sbase), "s"(lds) : "memory");
}
template <int C> DI void vadd_opq(unsigned& a) { asm volatile("v_add_u32 %0, %1, %0" : "+v"(a) : "i"(C)); }
template <int OFF> DI void gload16(u32x4& d, const void* sbase, unsigned voff) {
  asm volatile("global_load_dwordx4 %0, %1, %2 offset:%3" : "=v"(d) : "v"(voff), "s"(sbase), "i"(OFF) : "memory");
}
template <int OFF> DI void gload4(unsigned& d, const void* sbase, unsigned voff) {
  asm volatile("global_load_dword %0, %1, %2 offset:%3" : "=v"(d) : "v"(voff), "s"(sbase), "i"(OFF) : "memory");
}
template <int OFF> DI void gstore16_wt(void* sbase, unsigned voff, u32x4 v) {
  asm volatile("global_store_dwordx4 %0, %1, %2 offset:%3 sc1\n\ts_nop 1" ::"v"(voff), "v"(v), "s"(sbase), "i"(OFF) : "memory");
}
DI s16x4 trread(unsigned a) { return __builtin_amdgcn_ds_read_tr16_b64_v4i16(LDSP(s16x4, a)); }
DI bf16x8 cat8(s16x4 a, s16x4 b) { return __builtin_bit_cast(bf16x8, __builtin_shufflevector(a, b, 0, 1, 2, 3, 4, 5, 6, 7)); }
DI f32x16 mfma32(bf16x8 a, u32x4 b, f32x16 c) { return __builtin_amdgcn_mfma_f32_32x32x16_bf16(a, __builtin_bit_cast(bf16x8, b), c, 0, 0, 0); }
DI f32x4 mfma16(bf16x8 a, u32x4 b, f32x4 c) { return __builtin_amdgcn_mfma_f32_16x16x32_bf16(a, __builtin_bit_cast(bf16x8, b), c, 0, 0, 0); }

DI void mf(f32x4& c, const u32x4& a, const u32x4& b) { asm volatile("v_mfma_f32_16x16x32_bf16 %0, %1, %2, %0" : "+v"(c) : "v"(a), "v"(b)); }
DI void mf0(f32x4& c, const u32x4& a, const u32x4& b) { asm volatile("v_mfma_f32_16x16x32_bf16 %0, %1, %2, 0" : "=v"(c) : "v"(a), "v"(b)); }
template <int OFF> DI void trd(u32x2& d, unsigned a) { asm volatile("ds_read_b64_tr_b16 %0, %1 offset:%2" : "=v"(d) : "v"(a), "i"(OFF)); }
template <int OFF> DI void rd128(u32x4& d, unsigned a) { asm volatile("ds_read_b128 %0, %1 offset:%2" : "=v"(d) : "v"(a), "i"(OFF)); }
template <int V, int L> DI void wait_vl() {
  if constexpr (V >= 63) asm volatile("s_waitcnt lgkmcnt(%0)" ::"i"(L) : "memory");
  else asm volatile("s_waitcnt vmcnt(%0) lgkmcnt(%1)" ::"i"(V), "i"(L) : "memory");
}
DI void abar() { asm volatile("s_barrier" ::: "memory"); }
DI u32x4 mkrsrc(const void* p, unsigned bytes) { u32x4 r = {(unsigned)(size_t)p, (unsigned)((size_t)p >> 32) & 0xffffu, bytes, 0x20000u}; return r; }
template <int SO, int LO> DI void dma_set(unsigned& so, unsigned ldsbase) {
  asm volatile("s_add_u32 m0, %1, %3\n\ts_mov_b32 %0, %2" : "=s"(so) : "s"(ldsbase), "i"(SO), "i"(LO) : "scc");
}
DI void dma_setr(unsigned lds) { asm volatile("s_mov_b32 m0, %0" ::"s"(lds)); }
DI void dma_go(const u32x4& rs, unsigned voff, unsigned so) {
  asm volatile("buffer_load_dwordx4 %0, %1, %2 offen lds" ::"v"(voff), "s"(rs), "s"(so) : "memory");
}
template <class T> DI void opq_s(T& v) { asm volatile("" : "+s"(v)); }
template <class T> DI void opq_v(T& v) { asm volatile("" : "+v"(v)); }
DI u32x4 cat4(const u32x2& a, const u32x2& b) { return u32x4{a[0], a[1], b[0], b[1]}; }

DI void a_mul(float& d, float a, float b) { asm volatile("v_mul_f32 %0, %1, %2" : "=v"(d) : "v"(a), "v"(b)); }
DI void a_exp(float& d) { asm volatile("v_exp_f32 %0, %0" : "+v"(d)); }
DI void a_rcp(float& d) { asm volatile("v_rcp_f32 %0, %0" : "+v"(d)); }
DI void a_fma11(float& d, float s) { asm volatile("v_fma_f32 %0, %0, %1, %1" : "+v"(d) : "v"(s)); }
DI void a_cvt(unsigned& d, float a, float b) { asm volatile("v_cvt_pk_bf16_f32 %0, %1, %2" : "=v"(d) : "v"(a), "v"(b)); }
DI void a_pl16(unsigned& a, unsigned& b) { asm volatile("v_permlane16_swap_b32 %0, %1" : "+v"(a), "+v"(b)); }

template <int OFF> DI void wr128(unsigned a, const f32x4& v) { asm volatile("ds_write_b128 %0, %1 offset:%2" ::"v"(a), "v"(v), "i"(OFF) : "memory"); }
template <int OFF> DI void rd128f(f32x4& d, unsigned a) { asm volatile("ds_read_b128 %0, %1 offset:%2" : "=v"(d) : "v"(a), "i"(OFF)); }
DI void a_add(float& d, float a) { asm volatile("v_add_f32 %0, %0, %1" : "+v"(d) : "v"(a)); }
template <int OFF> DI void gload16_c(u32x4& d, const void* sbase, unsigned voff) {
  asm volatile("global_load_dwordx4 %0, %1, %2 offset:%3 sc1" : "=v"(d) : "v"(voff), "s"(sbase), "i"(OFF) : "memory");
}
template <int OFF> DI void gload8_c(u32x2& d, const void* sbase, unsigned voff) {
  asm volatile("global_load_dwordx2 %0, %1, %2 offset:%3 sc1" : "=v"(d) : "v"(voff), "s"(sbase), "i"(OFF) : "memory");
}
template <int OFF> DI void gload8(u32x2& d, const void* sbase, unsigned voff) {
  asm volatile("global_load_dwordx2 %0, %1, %2 offset:%3" : "=v"(d) : "v"(voff), "s"(sbase), "i"(OFF) : "memory");
}
template <int OFF> DI void gstore8_wt(void* sbase, unsigned voff, u32x2 v) {
  asm volatile("global_store_dwordx2 %0, %1, %2 offset:%3 sc1\n\ts_nop 1" ::"v"(voff), "v"(v), "s"(sbase), "i"(OFF) : "memory");
}
template <int V, int L> DI void wait_vl2() {
  if constexpr (V >= 63 && L >= 15) {}
  else if constexpr (V >= 63) asm volatile("s_waitcnt lgkmcnt(%0)" ::"i"(L) : "memory");
  else if constexpr (L >= 15) asm volatile("s_waitcnt vmcnt(%0)" ::"i"(V) : "memory");
  else asm volatile("s_waitcnt vmcnt(%0) lgkmcnt(%1)" ::"i"(V), "i"(L) : "memory");
}
DI unsigned umax3(unsigned a, unsigned b, unsigned c) { return __builtin_elementwise_max(__builtin_elementwise_max(a, b), c); }

namespace k1 {
#define K1_I0 2
#define K1_I1 3
#define K1_LAG 3
constexpr int NG = 6, GB = 18432, KO = NG * GB, VO = KO + 80 * 400, RO = VO + 80 * 144 + 64, LDSZ = RO + 320, PO = 2 * GB;
constexpr int mn(int a, int b) { return a < b ? a : b; }
constexpr int upto(int j) { return j < 0 ? K1_I1 : mn(14, mn(j >= 1 ? j - 1 + NG : NG, j + 1 + K1_LAG)); }
constexpr int ni(int g) { return g < 14 ? 9 * g : 9 * 13 + 5; }
}
DI float dot2(unsigned a, unsigned b, float c) { return __builtin_amdgcn_fdot2_f32_bf16(__builtin_bit_cast(bf16x2, a), __builtin_bit_cast(bf16x2, b), c, false); }
DI float xsum4(float v) {
  auto r = __builtin_amdgcn_permlane32_swap(__float_as_uint(v), __float_as_uint(v), false, false); v = __uint_as_float(r[0]) + __uint_as_float(r[1]);
  auto q = __builtin_amdgcn_permlane16_swap(__float_as_uint(v), __float_as_uint(v), false, false); return __uint_as_float(q[0]) + __uint_as_float(q[1]);
}
template <int C> DI float dpp_add(float v) { return v + __builtin_bit_cast(float, __builtin_amdgcn_update_dpp(0, __builtin_bit_cast(int, v), C, 0xf, 0xf, false)); }
DI float sum8(float v) { return dpp_add<0x141>(dpp_add<0x4e>(dpp_add<0xb1>(v))); }
DI float sum32(float v) { v = dpp_add<0x140>(sum8(v)); auto q = __builtin_amdgcn_permlane16_swap(__float_as_uint(v), __float_as_uint(v), false, false); return __uint_as_float(q[0]) + __uint_as_float(q[1]); }
template <int OFF> DI void wr128u(unsigned a, const u32x4& v) { asm volatile("ds_write_b128 %0, %1 offset:%2" ::"v"(a), "v"(v), "i"(OFF) : "memory"); }
template <int H> DI void k1_logits(float (&lg)[8], unsigned sb, int a, int lane, int tt) {
  const unsigned ka = sb + k1::KO + (lane + 2) * 400 + a * 48;
  const u32x4 q0 = *LDSP(u32x4, ka + 14 * 400 + 192), q1 = *LDSP(u32x4, ka + 14 * 400 + 208); const unsigned q2 = *LDSP(unsigned, ka + 14 * 400 + 224);
  for (int i = 4 * H; i < 4 * H + 4; ++i) {
    const u32x4 k0 = *LDSP(u32x4, ka + (7 - i) * 800), k1 = *LDSP(u32x4, ka + (7 - i) * 800 + 16); const unsigned k2 = *LDSP(unsigned, ka + (7 - i) * 800 + 32);
    float d = dot2(q2, k2, 0.f);
    for (int e = 0; e < 4; ++e) { d = dot2(q0[e], k0[e], d); d = dot2(q1[e], k1[e], d); }
    lg[i] = tt * 64 + lane - 2 * i < 0 ? -1e9f : d * 0.23570226039551584f; opq_v(lg[i]);
  }
}
DI void k1_softmax(float (&lg)[8], unsigned sb, int a, int lane) {
  for (int i = 0; i < 8; ++i) opq_v(lg[i]);
  const unsigned pa = sb + k1::PO + (a * 64 + lane) * 80;
  for (int i = 0; i < 4; ++i) *LDSP(volatile u32x4, pa + 16 * i) = u32x4{0u, 0u, 0u, 0u};
  float mx = lg[0];
  for (int i = 1; i < 8; ++i) mx = __builtin_fmaxf(mx, lg[i]);
  float sm = 0.f;
  for (int i = 0; i < 8; ++i) { lg[i] = __builtin_amdgcn_exp2f((lg[i] - mx) * 1.44269504088896f); sm += lg[i]; }
  const float rs = __builtin_amdgcn_rcpf(sm);
  for (int i = 0; i < 8; ++i) *LDSP(volatile u16, pa + (lane & 1) * 32 + (lane & 14) + 16 - 2 * i) = (u16)pk2(lg[i] * rs, 0.f);
}

struct K1Stage {
  u32x4 S[12];
  float mean[2], nmb[2], ssq[2];
  unsigned wa[2], lo, hi;
  template <int I> DI void centre() { constexpr int c = I >> 1, h = (c & 3) == 3, i = (I & 1) * 2;
    unsigned v0 = S[c][i], v1 = S[c][i + 1]; float t0, t1, t2, t3;
    asm volatile("v_dot2_f32_bf16 %3, %0, %7, %9\n\tv_dot2_f32_bf16 %4, %0, %8, %9\n\tv_dot2_f32_bf16 %5, %1, %7, %9\n\tv_dot2_f32_bf16 %6, %1, %8, %9\n\ts_nop 0\n\t"
                 "v_cvt_pk_bf16_f32 %0, %3, %4\n\tv_dot2c_f32_bf16 %2, %0, %0\n\tv_cvt_pk_bf16_f32 %1, %5, %6\n\tv_dot2c_f32_bf16 %2, %1, %1"
                 : "+v"(v0), "+v"(v1), "+v"(ssq[h]), "=&v"(t0), "=&v"(t1), "=&v"(t2), "=&v"(t3) : "s"(lo), "s"(hi), "v"(nmb[h]));
    S[c][i] = v0; S[c][i + 1] = v1; }
  template <int P> DI void put() { wr128u<0>(wa[0], S[4 * P]); wr128u<128>(wa[0], S[4 * P + 1]); wr128u<256>(wa[0], S[4 * P + 2]); wr128u<0>(wa[1], S[4 * P + 3]); }
  DI void fin(unsigned sb, int w, int lane) {
    float r[2];
    asm volatile("s_nop 2" : "+v"(ssq[0]), "+v"(ssq[1]));
    for (int h = 0; h < 2; ++h) {
      const float d = mean[h] + nmb[h], var = rbf(__builtin_fmaxf((h ? sum32((lane & 31) < 24 ? ssq[1] : 0.f) : sum8(ssq[0])) * (1.f / D_) - d * d, 0.f));
      r[h] = __builtin_amdgcn_rcpf(rbf(__builtin_sqrtf(rbf(var + 1e-5f))));
    }
    *LDSP(float, sb + k1::RO + ((lane & 1) ? 2 * w + (lane >> 5) : 16 + 8 * w + (lane >> 3)) * 4) = (lane & 1) ? r[1] : r[0];
  }
};

template <int R> DI void k1_gemm(K1Stage& st, const u16* __restrict__ Wo, char* __restrict__ ws, unsigned sb, int w, int lane, int tt STAMP_ARG) {
  using namespace k1;
  constexpr int NB = R < 2 ? 2 : 1, N1 = R == 0 ? 5 : R == 1 ? 4 : 3, N2 = R == 0 ? 2 : 3, T1 = R == 1 ? 5 : 0, T2 = R == 1 ? 2 : 0;
  constexpr int S1 = 18 * N1, NS = S1 + 18 * N2, PD = 3;
  const int a = w & 1;
  const int lrow = (R < 2 ? 16 + 32 * a : 0) + (lane & 15);
  u32x4 X[NB][18];
  const int t0 = R < 2 ? 0 : 2 * a;
  unsigned ra[2], xa = sb + KO + lrow * 416 + (lane >> 4) * 16;
  ra[0] = sb + ((lane >> 4) * 4 + ((lane & 15) >> 2)) * 288 + (lane & 3) * 8 + t0 * 32; ra[1] = ra[0] + 3 * GB;
  opq_v(ra[0]); opq_v(ra[1]); opq_v(xa);
  float rstd[NB];
  u32x2 rmv[9];
  const int rmr = 8 * blockIdx.x + 4 * a + (lane >> 4);
  if constexpr (R == 2) { const unsigned o = rmr < D_ ? rmr * (D_ * 2) + (lane & 15) * 8 : 0;
    sfor<0, 9>([&](auto ic) AINL { constexpr int i = decltype(ic)::v; gload8<i * 128>(rmv[i], Wo, o); }); }
  f32x4 acc[N1][NB], acv[N2][NB];
  u32x2 A[PD + 1][2];
  float lg[8];
  sfor<0, NS + PD>([&](auto sc) AINL {
    constexpr int s = decltype(sc)::v;
    if constexpr (s < NS) {
      constexpr int ks = s < S1 ? s / N1 : (s - S1) / N2, j = s < S1 ? s % N1 : (s - S1) % N2, g = s < S1 ? ks >> 1 : 9 + (ks >> 2);
      if constexpr (j == 0 && (s < S1 ? ks & 1 : ks & 3) == 0) {
        if constexpr (g == 0 || g == 6 || g == 10) asm volatile("s_waitcnt lgkmcnt(0)" ::: "memory");
        abar();
        if constexpr (g == 0) { STAMP_MID(2); }
        if constexpr (g == 9) { STAMP_MID(3); }
        if constexpr (g == 13) { STAMP_MID(4); }
        if constexpr (g == 1 || g == 3) { SCHED(); st.template put<(g + 1) / 2>(); }
        if constexpr (g == 4) st.fin(sb, w, lane);
      }
      if constexpr (g <= 4 && g % 2 == 0 && s - 2 * N1 * g < 3)
        sfor<0, 2 * NB>([&](auto ic) AINL { constexpr int k = 2 * (s - 2 * N1 * g) + decltype(ic)::v / NB, n = decltype(ic)::v % NB; rd128<n * 16 * 416 + k * 64>(X[n][3 * g + k], xa); });
      constexpr int off = (g % 3) * GB + (s < S1 ? (ks & 1) * 9216 + T1 * 32 : ((ks >> 1) & 1) * 9216 + (ks & 1) * 144 + T2 * 32) + j * 32;
      trd<off>(A[s % (PD + 1)][0], ra[g % NG / 3]); trd<off + 4608>(A[s % (PD + 1)][1], ra[g % NG / 3]);
      if constexpr (s < PD) st.template centre<8 + s>();
    }
    if constexpr (s >= PD) {
      constexpr int m = s - PD, ks = m < S1 ? m / N1 : (m - S1) / N2, j = m < S1 ? m % N1 : (m - S1) % N2;
      wait_vl2<63, 2 * (s < NS ? PD : NS - 1 - m)>();
      const u32x4 av = cat4(A[m % (PD + 1)][0], A[m % (PD + 1)][1]);
      sfor<0, NB>([&](auto bc) AINL { constexpr int n = decltype(bc)::v;
        if constexpr (m < S1) { if constexpr (ks == 0) mf0(acc[j][n], av, X[n][0]); else mf(acc[j][n], av, X[n][ks]); }
        else { if constexpr (ks == 0) mf0(acv[j][n], av, X[n][0]); else mf(acv[j][n], av, X[n][ks]); }
        });
      constexpr int c0 = 11 + (R < 2 ? m : m < 3 ? 2 * m : m + 3), c1 = mn(24, c0 + (R == 2 && m < 3 ? 2 : 1));
      sfor<c0, c1>([&](auto ic) AINL { st.template centre<decltype(ic)::v>(); });
      if constexpr (R == 2 && m == 10 * N1) {
        asm volatile("s_waitcnt vmcnt(0)" : "+v"(rmv[0]), "+v"(rmv[1]), "+v"(rmv[2]), "+v"(rmv[3]), "+v"(rmv[4]), "+v"(rmv[5]), "+v"(rmv[6]), "+v"(rmv[7]), "+v"(rmv[8])::"memory");
        float t = 0.f;
        for (int i = 0; i < 9; ++i) { t = dot2(rmv[i][0], 0x3f803f80u, t); t = dot2(rmv[i][1], 0x3f803f80u, t); }
        t = dpp_add<0x140>(sum8(t)) * (1.f / D_);
        const float hi = rbf(t);
        const unsigned v = pk2((lane & 1) ? t - hi : hi, 0.f);
        if ((lane & 15) < 2 && rmr < D_) asm volatile("global_store_short %0, %1, %2 sc1" ::"v"((lane & 1) * (D_ * 2) + rmr * 2), "v"(v), "s"(ws + WS_RM) : "memory");
      }
      if constexpr (m == S1 - 8) for (int n = 0; n < NB; ++n) { rstd[n] = *LDSP(float, sb + RO + (lrow + 16 * n) * 4); opq_v(rstd[n]); }
      if constexpr (m >= S1 && m < S1 + N1) {
        constexpr int t = m - S1;
        for (int n = 0; n < NB; ++n) {
          opq_v(acc[t][n]);
          for (int e = 0; e < 2; ++e) { const unsigned p = 8 * (t0 + T1 + t) + 2 * (lane >> 4) + e;
            *LDSP(unsigned, sb + KO + (lrow + 16 * n) * 400 + 4 * p + 12 * ((p * 57) >> 9)) = pk2(acc[t][n][2 * e] * rstd[n], acc[t][n][2 * e + 1] * rstd[n]); }
        }
      }
    }
    if constexpr (R == 2 && s == S1 + 4 * N2) k1_logits<0>(lg, sb, a, lane, tt);
    if constexpr (R == 2 && s == S1 + 8 * N2) k1_logits<1>(lg, sb, a, lane, tt);
    if constexpr (R == 2 && s == S1 + 12 * N2) k1_softmax(lg, sb, a, lane);
  });
  static_assert(N1 - 1 + PD < 4 * N2, "k, q are written before barrier 10");
  asm volatile("s_nop 7\n\ts_nop 3");
  sfor<0, N2 * NB>([&](auto jc) AINL { constexpr int t = decltype(jc)::v / NB, n = decltype(jc)::v % NB;
    opq_v(acv[t][n]);
    if (R == 0 || t < 2 || lane < 32 || (R == 2 && a == 0))
      *LDSP(u32x2, sb + VO + (lrow + 16 * n) * 144 + (t0 + T2 + t) * 32 + (lane >> 4) * 8) =
        u32x2{pk2(acv[t][n][0] * rstd[n], acv[t][n][1] * rstd[n]), pk2(acv[t][n][2] * rstd[n], acv[t][n][3] * rstd[n])}; });
}

template <int VB> DI void k1_stage0(K1Stage& st, char* __restrict__ ws, unsigned sb, int w, int lane, int tt, int hg) {
  float sum[3] = {0.f, 0.f, 0.f};
  unsigned on = (lane & 31) < 24 ? 0x3f803f80u : 0u;
  st.lo = 0x3f80u; st.hi = 0x3f800000u; opq_s(st.lo); opq_s(st.hi);
  st.wa[0] = sb + k1::KO + (16 + 8 * w + (lane >> 3)) * 416 + (lane & 7) * 16; st.wa[1] = sb + k1::KO + (2 * w + (lane >> 5)) * 416 + min(lane & 31, 23) * 16;
  sfor<0, 12>([&](auto kc) AINL { constexpr int k = decltype(kc)::v;
    if constexpr (k % 2 == 0) asm volatile("s_waitcnt vmcnt(%5)" : "+v"(st.S[k]), "+v"(st.S[k + 1]), "+v"(sum[0]), "+v"(sum[1]), "+v"(sum[2]) : "i"(VB + 10 - k) : "memory");
    for (int i = 0; i < 4; ++i) { if constexpr ((k & 3) == 3) sum[2] = dot2(st.S[k][i], on, sum[2]); else sum[i & 1] = dot2(st.S[k][i], 0x3f803f80u, sum[i & 1]); } });
  st.mean[0] = sum8(sum[0] + sum[1]) * (1.f / D_); st.mean[1] = sum32(sum[2]) * (1.f / D_);
  for (int h = 0; h < 2; ++h) { st.nmb[h] = -rbf(st.mean[h]); st.ssq[h] = 0.f; }
  sfor<0, 8>([&](auto ic) AINL { st.template centre<decltype(ic)::v>(); });
  st.template put<0>();
  if (VB == 0 && hg == 0 && (lane & 7) == 0) asm volatile("global_store_dword %0, %1, %2 sc1" ::"v"(lane >> 1), "v"(st.mean[0]), "s"(ws + WS_MEANX + (tt * 8 + w) * 32) : "memory");
}

__global__ __launch_bounds__(512, 2) void k1_attn(const u16* __restrict__ x, const u16* __restrict__ Wq, const u16* __restrict__ Wo, char* __restrict__ ws) {
  using namespace k1;
  __shared__ __attribute__((aligned(1024))) char smem[LDSZ];
  STAMP_BEGIN();
  const unsigned sb = (unsigned)(size_t)smem;
  const int w = __builtin_amdgcn_readfirstlane(threadIdx.x >> 6);
  const int lane = threadIdx.x & 63;
  const int b = blockIdx.x, bi = b >> 3;
  const int hg = (b & 1) * 4 + (bi & 3), tt = ((b >> 1) & 3) * 8 + (bi >> 2);
  K1Stage st;
  auto ldx = [&]() AINL {
    const unsigned o0 = (tt * 64 + 8 * w + (lane >> 3)) * (D_ * 2) + (lane & 7) * 16, o1 = max(tt * 64 - 16 + 2 * w + (lane >> 5), 0) * (D_ * 2) + min(lane & 31, 23) * 16;
    sfor<0, 12>([&](auto kc) AINL { constexpr int k = decltype(kc)::v;
      if constexpr ((k & 3) == 3) gload16<(k >> 2) * 384>(st.S[k], x, o1); else gload16<(k >> 2) * 384 + (k & 3) * 128>(st.S[k], x, o0); }); };
  if (w < 2) { ldx(); k1_stage0<0>(st, ws, sb, w, lane, tt, hg); STAMP_MID(1); k1_gemm<0>(st, Wo, ws, sb, w, lane, tt STAMP_PASS); }
  else if (w < 4) { ldx(); k1_stage0<0>(st, ws, sb, w, lane, tt, hg); STAMP_MID(1); k1_gemm<1>(st, Wo, ws, sb, w, lane, tt STAMP_PASS); }
  else if (w < 6) { ldx(); k1_stage0<0>(st, ws, sb, w, lane, tt, hg); STAMP_MID(1); k1_gemm<2>(st, Wo, ws, sb, w, lane, tt STAMP_PASS); }
  else {
    ldx();
    const u32x4 rsW = mkrsrc(Wq, D_ * 3456);
    unsigned vo[9], so, dst = sb + (w & 1) * 1024;
    auto issue = [&](auto gc) AINL {
      constexpr int g = decltype(gc)::v;
      sfor<0, (g < 13 ? 9 : 5)>([&](auto mc) AINL { constexpr int m = decltype(mc)::v;
        if constexpr (g == 0 || g == 9) {
          const unsigned id = 128 * m + 64 * (w & 1) + lane, P = (id * 3641) >> 16, c = id - 18 * P, kr = 8 * ((P >> 2) & 3) + 4 * ((P >> 4) & 1) + (P & 3);
          if constexpr (g == 0) vo[m] = (kr + (P & 32)) * 3456 + 16 * c + (c < 9 ? 1152 : -144) + 144 * hg;
          else vo[m] = (kr + 2 * (P & 32)) * 3456 + 16 * c + (c < 9 ? 2304 : 32 * 3456 + 2160) + 144 * hg;
          opq_v(vo[m]);
        }
        dma_set<(g < 9 ? g * 64 : (g - 9) * 128) * 3456, (g % NG) * GB + m * 2048>(so, dst); dma_go(rsW, vo[m], so); });
    };
    sfor<0, K1_I0>(issue);
    k1_stage0<9 * K1_I0>(st, ws, sb, w, lane, tt, hg);
    sfor<K1_I0, upto(-1)>(issue);
    float lg[8];
    sfor<0, 14>([&](auto gc) AINL { constexpr int g = decltype(gc)::v;
      wait_vl2<ni(upto(g - 1)) - ni(g + 1), 0>();
      abar();
      sfor<upto(g - 1), upto(g)>(issue);
      if constexpr (g == 0 || g == 2) sfor<8 + 4 * g, 16 + 4 * g>([&](auto ic) AINL { st.template centre<decltype(ic)::v>(); });
      if constexpr (g == 1 || g == 3) st.template put<(g + 1) / 2>();
      if constexpr (g == 4) st.fin(sb, w, lane);
      if constexpr (g == 10) k1_logits<0>(lg, sb, w & 3, lane, tt);
      if constexpr (g == 11) k1_logits<1>(lg, sb, w & 3, lane, tt);
      if constexpr (g == 12) k1_softmax(lg, sb, w & 3, lane);
    });
    if (hg == 0 && (lane & 7) == 0) asm volatile("global_store_dword %0, %1, %2 sc1" ::"v"(lane >> 1), "v"(st.mean[0]), "s"(ws + WS_MEANX + (tt * 8 + w) * 32) : "memory");
  }
  const int hf = w & 1, g = lane >> 4;
  unsigned va = sb + VO + ((w >> 1) * 16 + (g & 1) * 16 + ((lane >> 1) & 6) + (g >> 1)) * 144 + (lane & 3) * 8 + hf * 64;
  unsigned pa = sb + PO + ((w >> 1) * 16 + (lane & 15)) * 80 + g * 16, pa1 = pa + hf * 5120, va2 = va + (hf ? 32 : 128);
  char* ob = ws + WS_OF + (size_t)(tt * 4 + (w >> 1)) * 18432 + hg * 2304;
  opq_v(va); opq_v(pa); opq_v(pa1); opq_v(va2); opq_s(ob);
  asm volatile("s_waitcnt lgkmcnt(0)" ::: "memory");
  abar();
  STAMP_MID(5);
  u32x2 V[3][2]; u32x4 P[3]; f32x4 o[5];
  rd128<0>(P[0], pa1); trd<0>(V[0][0], va); trd<1152>(V[0][1], va); rd128<5120>(P[1], pa1); trd<32>(V[1][0], va); trd<1184>(V[1][1], va);
  rd128<15360>(P[2], pa); trd<0>(V[2][0], va2); trd<1152>(V[2][1], va2);
  wait_vl2<63, 6>(); mf0(o[0], cat4(V[0][0], V[0][1]), P[0]);
  wait_vl2<63, 5>(); mf0(o[1], cat4(V[0][0], V[0][1]), P[1]);
  wait_vl2<63, 3>(); mf0(o[2], cat4(V[1][0], V[1][1]), P[0]); mf0(o[3], cat4(V[1][0], V[1][1]), P[1]);
  wait_vl2<63, 0>(); mf0(o[4], cat4(V[2][0], V[2][1]), P[2]);
  asm volatile("s_nop 7\n\ts_nop 3" : "+v"(o[0]), "+v"(o[1]), "+v"(o[2]), "+v"(o[3]), "+v"(o[4]));
  unsigned q[5][2];
  for (int t = 0; t < 5; ++t) for (int e = 0; e < 2; ++e) q[t][e] = pk2(o[t][2 * e], o[t][2 * e + 1]);
  u32x4 ov;
  for (int e = 0; e < 2; ++e) {
    const unsigned c0 = hf && g > 0 ? q[1][e] : q[0][e], c1 = hf ? (2 * g + e < 3 ? q[3][e] : q[4][e]) : (2 * g + e < 1 ? q[2][e] : q[3][e]);
    auto r = __builtin_amdgcn_permlane16_swap(c0, c1, false, false); ov[e] = r[0]; ov[2 + e] = r[1];
  }
  gstore16_wt<0>(ob, (hf * 4 + (g & 1) * 2 + (g >> 1)) * 256 + (lane & 15) * 16, ov);
  if (!hf && lane < 32) gstore8_wt<2048>(ob, (lane & 15) * 16 + g * 8, u32x2{q[4][0], q[4][1]});
  STAMP_END(ws, 0);
}

__global__ __launch_bounds__(512, 2) void k2_proj(const u16* __restrict__ x, const u16* __restrict__ Wo, char* __restrict__ ws) {
  __shared__ __attribute__((aligned(1024))) char smem[73728 + 3072];
  STAMP_BEGIN();
  const unsigned sb = (unsigned)(size_t)smem;
  const int w = __builtin_amdgcn_readfirstlane(threadIdx.x >> 6);
  const int lane = threadIdx.x & 63;
  const int b = blockIdx.x;
  const int lin = (b & 7) * 29 + (b >> 3) + ((b & 7) < 2 ? (b & 7) : 2);
  const int tt = lin / 9, cg = lin - tt * 9;
  if (w >= 5) {
    const u32x4 rsW = mkrsrc(Wo, D_ * D_ * 2);
    unsigned vo[4];
    for (int i = 0; i < 4; ++i) { const int p = i * 8 + (lane >> 3), k = 8 * ((p >> 2) & 3) + 4 * (p >> 4) + (p & 3);
      vo[i] = (k + 32 * (w - 5)) * (D_ * 2) + ((((lane >> 1) & 3) ^ ((p >> 1) & 3)) * 32) + (lane & 1) * 16 + cg * 128; }
    unsigned dst = sb + (w - 5) * 4096, so;
    { const u32x4 rsR = mkrsrc(ws + WS_RM, 3072); dma_set<0, 73728>(so, sb + (w - 5) * 1024); dma_go(rsR, lane * 16 + (w - 5) * 1024, so); }
    sfor<0, 24>([&](auto ic) AINL { constexpr int i = decltype(ic)::v;
      dma_set<(i >> 2) * 96 * (D_ * 2), (i >> 2) * 12288 + (i & 3) * 1024>(so, dst); dma_go(rsW, vo[i & 3], so); });
    sfor<0, 6>([&](auto sc) AINL { wait_vm<20 - 4 * decltype(sc)::v>(); abar(); });
    return;
  }
  const int tb = min(tt * 5 + w, 127);
  u32x4 X[18], xr[2];
  { const char* ob = ws + WS_OF + (size_t)tb * 18432 + 4096;
    sfor<0, 18>([&](auto kc) AINL { constexpr int k = decltype(kc)::v; gload16<(k & 7) * 1024 - 4096>(X[k], ob + (k >> 3) * 8192, lane * 16); }); }
  { const char* xb = (const char*)x + (size_t)tb * 16 * (D_ * 2) + cg * 128; const unsigned o = (lane & 15) * (D_ * 2) + (lane >> 4) * 16;
    gload16<0>(xr[0], xb, o); gload16<64>(xr[1], xb, o); }
  unsigned mx;
  gload4<0>(mx, ws + WS_MEANX + tb * 64, (lane & 15) * 4);
  unsigned ra[4];
  const int rrow = (lane >> 4) * 4 + ((lane & 15) >> 2);
  for (int t = 0; t < 4; ++t) ra[t] = sb + rrow * 128 + ((t ^ ((rrow >> 1) & 3)) * 32) + (lane & 3) * 8;
  const unsigned rm = sb + 73728 + (lane & 1) * (D_ * 2) + (lane >> 4) * 16;
  u32x4 s0, s1;
  { const unsigned one = 0x3f80u << (16 * (lane & 1)); const int d = (lane & 7) >> 1, hit = (lane >> 4) - ((lane >> 3) & 1);
    for (int i = 0; i < 4; ++i) { s0[i] = (i == d && hit == 0) ? one : 0u; s1[i] = (i == d && hit == 2) ? one : 0u; }
    opq_v(s0); opq_v(s1); }
  f32x4 acc[5];
  u32x2 A[2][4][2];
  u32x4 Ar[2];
  sfor<0, 19>([&](auto kc) AINL {
    constexpr int k = decltype(kc)::v;
    if constexpr (k % 3 == 0 && k < 18) abar();
    if constexpr (k == 0) { STAMP_MID(1); }
    if constexpr (k == 15) { STAMP_MID(2); }
    if constexpr (k == 16) for (int t = 0; t < 4; ++t) vadd_opq<65536>(ra[t]);
    sfor<0, 5>([&](auto tc) AINL {
      constexpr int t = decltype(tc)::v;
      if constexpr (k < 18 && t < 4) { trd<(k & 15) * 4096>(A[k & 1][t][0], ra[t]); trd<(k & 15) * 4096 + 2048>(A[k & 1][t][1], ra[t]); }
      if constexpr (k < 18 && t == 4) rd128<k * 64>(Ar[k & 1], rm);
      if constexpr (k > 0) {
        wait_vl2<(t == 0 ? 21 - k : 63), (k < 18 ? 9 : t < 4 ? 7 - 2 * t : 0)>();
        if constexpr (t < 4) { const u32x4 av = cat4(A[~k & 1][t][0], A[~k & 1][t][1]); if constexpr (k == 1) mf0(acc[t], av, X[0]); else mf(acc[t], av, X[k - 1]); }
        else { if constexpr (k == 1) mf0(acc[4], Ar[~k & 1], X[0]); else mf(acc[4], Ar[~k & 1], X[k - 1]); }
      }
    });
  });
  STAMP_MID(3);
  asm volatile("s_waitcnt vmcnt(0)" : "+v"(xr[0]), "+v"(xr[1]), "+v"(mx)::"memory");
  for (int t = 0; t < 4; ++t) mf(acc[t], (t & 1) ? s1 : s0, xr[t >> 1]);
  asm volatile("s_nop 7\n\ts_nop 3" : "+v"(acc[0]), "+v"(acc[1]), "+v"(acc[2]), "+v"(acc[3]), "+v"(acc[4]));
  const float mean = __uint_as_float(mx) + (acc[4][0] + acc[4][1]), mb = rbf(mean);
  u32x4 hf[2], hc[2];
  float ss = 0.f;
  for (int t = 0; t < 4; ++t)
    for (int i = 0; i < 2; ++i) {
      const unsigned p = pk2(acc[t][2 * i], acc[t][2 * i + 1]);
      const float a = lo16(p), c = hi16(p);
      hf[t >> 1][(t & 1) * 2 + i] = p;
      hc[t >> 1][(t & 1) * 2 + i] = pk2(a - mb, c - mb);
      ss = __builtin_fmaf(a - mean, a - mean, ss); ss = __builtin_fmaf(c - mean, c - mean, ss);
    }
  { auto r = __builtin_amdgcn_permlane32_swap(__float_as_uint(ss), __float_as_uint(ss), false, false); ss = __uint_as_float(r[0]) + __uint_as_float(r[1]);
    auto q = __builtin_amdgcn_permlane16_swap(__float_as_uint(ss), __float_as_uint(ss), false, false); ss = __uint_as_float(q[0]) + __uint_as_float(q[1]); }
  if (tt * 5 + w < 128) {
    const size_t fo = ((size_t)tb * 18 + cg * 2) * 1024;
    gstore16_wt<0>(ws + WS_HC + fo, lane * 16, hc[0]); gstore16_wt<1024>(ws + WS_HC + fo, lane * 16, hc[1]);
    if (lane < 16) asm volatile("global_store_dword %0, %1, %2 sc1" ::"v"(lane * 4), "v"(ss), "s"(ws + WS_SSQ + cg * 8192 + tb * 64) : "memory");
    gstore16_wt<0>(ws + WS_HF + fo, lane * 16, hf[0]); gstore16_wt<1024>(ws + WS_HF + fo, lane * 16, hf[1]);
  }
  STAMP_END(ws, 1);
}

namespace k4 {
#define K4_N0 6
#define K4_LA 24
#define K4_FAST 2
#define K4_UPL 150
constexpr int N0 = K4_N0, UA = 18 * N0, UB = 126, UE = 162, NR = 41, RR_ = N0 == 7 ? 13 : 14, RING = 8 * RR_, RBO = RING * 1024, RBS = N0 * 1024, LA = K4_LA, UPL = K4_UPL;
constexpr int slot_of(int u) { return u < UA ? u % N0 : u < UB ? 6 : 7 + ((u - UB) & 1); }
constexpr int ks_of(int u) { return u < UA ? u / N0 : u < UB ? u - UA : (u - UB) >> 1; }
}

namespace k3 {
constexpr int NSLOT = 7, C2OFF = NSLOT * 16384;
constexpr int U1 = 144, U2 = 180, U3 = 252, UE = 324;
enum { OW = 0, OX = 1, OC2 = 2, OSQ = 3, OST = 4, OPD = 5, OPS = 6, OFL = 7 };
constexpr int upd(int i) { return 20 + 2 * i; }
constexpr int upr(int i) { return 52 + 16 * i; }
constexpr int ups(int i) { return 56 + 16 * i; }
constexpr int ufl(int i) { return 30 + 16 * i; }
enum { VM1 = 0, VEX, VFM, VRC, VM2, VCV, VPL };
struct Sched {
  int n; int Q[160]; int kind[160]; int idx[160];
  int nv; int VQ[400]; int vop[400]; int vsl[400]; int vix[400];
  int stq[7];
  constexpr void add(int q, int k, int i) { Q[n] = q; kind[n] = k; idx[n] = i; ++n; }
  static constexpr int ubar(int j) { return j <= 10 ? 16 * j + 13 : 177 + 16 * (j - 10); }
  static constexpr int uw(int L, int h) {
    if (L <= 8) return 16 * (L - 2) + 2 * h;
    if (L <= 13) return 140 + 8 * (L - 9) + 2 * h;
    return 186 + 16 * (L - 14) + 2 * h;
  }
  static constexpr int uc2(int i) { return i == 0 ? 100 : i == 1 ? 110 : i == 2 ? 118 : i == 3 ? 126 : 134; }
  constexpr int glu(int q, int a, int b, int cap) {
    int used = 0;
    for (int op = VM1; op <= VCV; ++op)
      for (int t = 0; t < (b >= 0 ? 2 : 1); ++t)
        for (int v = 0; v < (op == VCV ? 4 : 8); ++v) {
          int c = (op == VEX || op == VRC) ? 8 : 4;
          if (used && used + c > cap) { ++q; used = 0; }
          VQ[nv] = q; vop[nv] = op; vsl[nv] = t ? b : a; vix[nv] = v; ++nv; used += c;
        }
    return q + 1;
  }
  constexpr Sched() : n(0), Q{}, kind{}, idx{}, nv(0), VQ{}, vop{}, vsl{}, vix{}, stq{} {
    int q = glu(2 * U1 + 2, 0, 1, 8);
    stq[0] = (q + 1) & ~1; stq[1] = stq[0] + 4;
    q = glu(2 * U2 + 2, 2, 3, 4);
    stq[2] = (q + 1) & ~1; stq[3] = stq[2] + 16;
    q = glu(q, 4, -1, 4);
    for (int v = 0; v < 2; ++v) { VQ[nv] = q + 1 + v; vop[nv] = VPL; vsl[nv] = 4; vix[nv] = v; ++nv; }
    stq[4] = (q + 5) & ~1;
    q = glu(2 * U3 + 8, 0, 1, 4);
    stq[5] = (q + 1) & ~1; stq[6] = stq[5] + 16;
    add(-1, OW, 0); add(-1, OW, 1);
    for (int k = 0; k < 4; ++k) add(-1, OX, k);
    for (int i = 0; i < 5; ++i) add(-1, OSQ, i);
    add(-1, OW, 2); add(-1, OW, 3);
    for (int k = 4; k < 8; ++k) add(-1, OX, k);
    for (int qq = 0; qq < 2 * UE; ++qq) {
      int u = qq >> 1, h = qq & 1;
      if (h == 1) for (int L = 2; L < 18; ++L) for (int p = 0; p < 2; ++p) if (uw(L, p) == u) add(qq, OW, 2 * L + p);
      if (h == 1) for (int i = 0; i < 5; ++i) if (uc2(i) == u) add(qq, OC2, i);
      if (h == 0 && u < 16 * 7) { int r = u & 15; if (r == 5 || r == 7 || r == 9 || r == 11) add(qq, OX, 4 * ((u >> 4) + 2) + (r - 5) / 2); }
      for (int t = 0; t < 7; ++t) if (stq[t] == qq) add(qq, OST, t);
      if (h == 1) for (int i = 0; i < 3; ++i) { if (upd(i) == u) add(qq, OPD, i); if (ups(i) == u) add(qq, OPS, i); }
      if (h == 1) for (int i = 0; i < 4; ++i) if (ufl(i) == u) add(qq, OFL, i);
    }
  }
  constexpr int find(int k, int i) const { for (int j = 0; j < n; ++j) if (kind[j] == k && idx[j] == i) return j; return -1000; }
  constexpr int cnt(int q, int k, int i) const { int j = find(k, i), c = 0; for (int m = j + 1; m < n; ++m) if (Q[m] < q) ++c; return c; }
  constexpr int barat(int u) const { for (int j = 0; j < 19; ++j) if (ubar(j) == u) return j; return -1; }
  constexpr int vmat(int u) const {
    int v = 63;
    if (u < U1 && (u & 7) == 0) v = cnt(2 * u, OX, 2 * (u >> 3) + 1);
    int j = barat(u);
    if (j >= 0) { int c = j < 8 ? cnt(2 * u, OW, 2 * (j + 1) + 1) : j == 8 ? cnt(2 * u, OC2, 1) : j == 9 ? cnt(2 * u, OC2, 3) : cnt(2 * u, OW, 2 * (j - 1) + 1); if (c < v) v = c; }
    return v;
  }
  constexpr int opat(int q, int k) const { for (int j = 0; j < n; ++j) if (Q[j] == q && kind[j] == k) return idx[j]; return -1; }
  constexpr int vfirst(int q) const { for (int j = 0; j < nv; ++j) if (VQ[j] >= q) return j; return nv; }
};
constexpr Sched SC{};
static_assert(SC.stq[1] < 2 * U2 && SC.stq[4] < 2 * U3 + 8 && SC.stq[6] < 2 * UE, "schedule");
}

__global__ __launch_bounds__(512, 2) void k3_gateup(const u16* __restrict__ Wg, const u16* __restrict__ Wu, const u16* __restrict__ Wd, char* __restrict__ ws) {
  using namespace k3;
  __shared__ __attribute__((aligned(1024))) char smem[C2OFF + 36864];
  STAMP_BEGIN();
  const unsigned sb = (unsigned)(size_t)smem;
  const int w = __builtin_amdgcn_readfirstlane(threadIdx.x >> 6);
  const int lane = threadIdx.x & 63;
  const int b = blockIdx.x, xcd = b & 7, bi = b >> 3;
  const int tt = (xcd >> 2) * 4 + (bi >> 3);
  const int fg = (xcd & 3) * 8 + (bi & 7);
  const int G = fg >> 2, j = fg & 3;
  const int tb = tt * 16 + 2 * w;
  const int gu = w >> 2;
  const u32x4 rsW = mkrsrc(gu ? Wu : Wg, D_ * F_ * 2);
  unsigned wlo, w2lo;
  { int row = lane >> 3, pos = (lane >> 1) & 3; int tile = pos ^ ((row >> 1) & 3);
    wlo = (row + 16 * (w & 3)) * (F_ * 2) + tile * 32 + (lane & 1) * 16 + (G * 9 + 2 * j) * 128;
    w2lo = ((lane >> 1) + 32 * (w & 3)) * (F_ * 2) + (lane & 1) * 16 + (G * 9 + 8) * 128 + j * 32; }
  unsigned wdst = sb + gu * 8192 + (w & 3) * 2048;
  unsigned wdst2 = sb + gu * 4096 + (w & 3) * 1024;
  unsigned c2dst = sb + C2OFF + gu * 18432 + (w & 3) * 1024;
  u32x4 X[36];
  unsigned sq[5];
  const char* xb[2][3];
  for (int k = 0; k < 2; ++k) for (int i = 0; i < 3; ++i) { xb[k][i] = ws + WS_HC + (size_t)(tb + k) * 18432 + 4096 + i * 8192; opq_s(xb[k][i]); }
  char* ab[2];
  for (int k = 0; k < 2; ++k) { ab[k] = ws + WS_ACT + (size_t)(tb + k) * 144 * 1024 + (size_t)(G * 18 + 4 * j) * 1024; opq_s(ab[k]); }
  char* ab2 = ws + WS_ACT + (size_t)tb * 144 * 1024 + (size_t)(G * 18 + 16 + (j >> 1)) * 1024 + (j & 1) * 512; opq_s(ab2);
  const unsigned l16 = lane * 16;
  char* fb = ws + WS_PART + (size_t)b * PART_BLK + w * 3072; opq_s(fb);
  char* fb2 = ws + WS_PART + (size_t)b * PART_BLK + 24576 + w * 384; opq_s(fb2);
  unsigned so;
  float sdv[2], nav[2];
  f32x4 acc[5][4];
  float gv[5][8];
  unsigned ov[5][4];
  unsigned pso[3], pdo[3], pvo, pk_rd;
  u32x4 rsD, rsP, pkv, ones;
  auto vm_set = [&](auto Qc) AINL {
    constexpr int q = decltype(Qc)::v, iw = SC.opat(q, OW), ic = SC.opat(q, OC2), ip = SC.opat(q, OPD);
    if constexpr (ip >= 0) {
      const int p = (w & 3) * 8 + (lane >> 3), kn = 8 * ((p >> 2) & 3) + 4 * (p >> 4) + (p & 3);
      pvo = ((pso[ip] & 1) ? kn : p) * (D_ * 2) + ((((lane >> 1) & 3) ^ ((p >> 1) & 3)) * 32) + (lane & 1) * 16; opq_v(pvo);
      pso[ip] &= ~1u; opq_s(pso[ip]);
      dma_set<0, C2OFF + ip * 8192>(so, sb + w * 1024);
    }
    if constexpr (iw >= 0 && iw < 18) dma_set<((iw >> 1) * 64 + (iw & 1) * 8) * (F_ * 2), ((iw >> 1) % NSLOT) * 16384 + (iw & 1) * 1024>(so, wdst);
    if constexpr (iw >= 18) { constexpr int hs = iw - 18;
      dma_set<(hs % 9) * 64 * (F_ * 2) + (hs / 9) * 64, ((iw >> 1) % NSLOT) * 16384 + (iw & 1) * 8192>(so, wdst2); }
    if constexpr (ic >= 0) dma_set<ic * 128 * (F_ * 2), ic * 4096>(so, c2dst);
  };
  auto vm_go = [&](auto Qc) AINL {
    constexpr int q = decltype(Qc)::v, iw = SC.opat(q, OW), ic = SC.opat(q, OC2), ix = SC.opat(q, OX), is = SC.opat(q, OST);
    if constexpr (iw >= 0) dma_go(rsW, wlo, so);
    if constexpr (ic >= 0) dma_go(rsW, w2lo, so);
    constexpr int ipd = SC.opat(q, OPD), ips = SC.opat(q, OPS);
    if constexpr (ipd >= 0) dma_go(rsD, pvo, pso[ipd]);
    if constexpr (ips >= 0) asm volatile("buffer_store_dwordx4 %0, %1, %2, %3 offen sc1\n\ts_nop 1" ::"v"(pkv), "v"(l16), "s"(rsP), "s"(pdo[ips]) : "memory");
    if constexpr (SC.opat(q, OFL) >= 0) {
      constexpr int i = SC.opat(q, OFL);
      if constexpr (i == 0) { ones = u32x4{~0u, ~0u, ~0u, ~0u}; opq_v(ones); }
      if constexpr (i < 3) gstore16_wt<i * 1024>(fb, l16, ones);
      else gstore16_wt<0>(fb2, min(l16, 368u), ones);
    }
    if constexpr (ix >= 0) gload16<((ix >> 1) & 7) * 1024 - 4096>(X[ix], xb[ix & 1][ix >> 4], l16);
    if constexpr (is == 4) {
      const unsigned off = ((lane >> 4) & 1) * (144 * 1024) + (lane >> 5) * 256 + (lane & 15) * 16;
      gstore16_wt<0>(ab2, off, u32x4{ov[4][0], ov[4][1], ov[4][2], ov[4][3]});
    } else if constexpr (is >= 0) {
      constexpr int i2 = is > 4 ? is - 1 : is, pr = i2 >> 1, tk = i2 & 1, a = (pr & 1) * 2;
      gstore16_wt<pr * 1024>(ab[tk], l16, u32x4{ov[a][2 * tk], ov[a][2 * tk + 1], ov[a + 1][2 * tk], ov[a + 1][2 * tk + 1]});
    }
  };
  auto vop = [&](auto opc, auto slc, auto vc) AINL {
    constexpr int op = decltype(opc)::v, sl = decltype(slc)::v, v = decltype(vc)::v, tk = v >> 2;
    if constexpr (op == VM1) a_mul(gv[sl][v], acc[sl][tk][v & 3], nav[tk]);
    else if constexpr (op == VEX) a_exp(gv[sl][v]);
    else if constexpr (op == VFM) a_fma11(gv[sl][v], sdv[tk]);
    else if constexpr (op == VRC) a_rcp(gv[sl][v]);
    else if constexpr (op == VM2) a_mul(gv[sl][v], gv[sl][v], acc[sl][2 + tk][v & 3]);
    else if constexpr (op == VCV) a_cvt(ov[sl][v], gv[sl][2 * v], gv[sl][2 * v + 1]);
    else a_pl16(ov[sl][v], ov[sl][2 + v]);
  };
  auto valu = [&](auto Qc) AINL {
    constexpr int q = decltype(Qc)::v;
    sfor<SC.vfirst(q), SC.vfirst(q + 1)>([&](auto jc) AINL { constexpr int jj = decltype(jc)::v; vop(IC<SC.vop[jj]>{}, IC<SC.vsl[jj]>{}, IC<SC.vix[jj]>{}); });
  };
  sfor<0, 17>([&](auto jc) AINL {
    constexpr int jj = decltype(jc)::v, kd = SC.kind[jj], id = SC.idx[jj];
    static_assert(SC.Q[jj] == -1 && SC.Q[17] >= 0, "prologue");
    if constexpr (kd == OW) { dma_set<((id >> 1) * 64 + (id & 1) * 8) * (F_ * 2), (id >> 1) * 16384 + (id & 1) * 1024>(so, wdst); dma_go(rsW, wlo, so); }
    else if constexpr (kd == OX) gload16<((id >> 1) & 7) * 1024 - 4096>(X[id], xb[id & 1][id >> 4], l16);
    else if constexpr (kd == OSQ) gload4<0>(sq[id], ws + WS_SSQ + (size_t)tb * 64 + id * 2 * 8192, (lane & 31) * 4 + (lane >> 5) * 8192);
  });

  rsD = mkrsrc(Wd, b < 216 ? D_ * F_ * 2 : 0); rsP = mkrsrc(ws + WS_WDP, b < 216 ? D_ * F_ * 2 : 0);
  { const unsigned n = 6 * b + 2 * (lane & 3) + (w >> 2), ksg = n / 9, T = (n - ksg * 9) * 4 + (w & 3);
    const unsigned q = ksg / 36, kk = ksg - q * 36, kh = kk >= 18, ks = kk - 18 * kh, cg = T / 9, lt = T - cg * 9 - (cg & 1);
    const unsigned sl = (((lt >> 1) - q - 1) & 3) * 2 + (lt & 1);
    const unsigned u = lt >= 8 ? (k4::N0 == 7 ? ks * 7 + 6 : k4::UA + ks) : sl < 6 ? ks * k4::N0 + sl : k4::UB + 2 * ks + sl - 6;
    const unsigned a = ksg * 32 * (D_ * 2) + (n - ksg * 9) * 128 + (ks >= 16), d = ((q * 4 + cg) * 324 + 2 * u + kh) * 1024;
    for (int i = 0; i < 3; ++i) { pso[i] = __builtin_amdgcn_readlane(a, i); pdo[i] = __builtin_amdgcn_readlane(d, i); opq_s(pso[i]); opq_s(pdo[i]); } }
  opq_s(rsD); opq_s(rsP);
  unsigned ra[4];
  const int rrow = (lane >> 4) * 4 + ((lane & 15) >> 2);
  for (int t = 0; t < 4; ++t) ra[t] = sb + rrow * 128 + ((t ^ ((rrow >> 1) & 3)) * 32) + (lane & 3) * 8;
  u32x2 A[4][2];
  auto ldA = [&](auto uc, auto hc) AINL {
    constexpr int u = decltype(uc)::v, h = decltype(hc)::v;
    if constexpr (u < U1) {
      constexpr int t = u & 3, g = u >> 4, k = (u >> 2) & 3, slot = g % NSLOT;
      trd<(slot & 3) * 16384 + (k & 1) * 8192 + (k >> 1) * 4096 + h * 2048>(A[u & 3][h], ra[t]);
    } else if constexpr (u < U2) {
      constexpr int n = u - U1;
      trd<(n & 1) * 18432 + (n >> 1) * 1024 + h * 512>(A[u & 3][h], ra[0]);
    } else if constexpr (u < UE) {
      constexpr int n = u - U2, slot = (9 + (n >> 4)) % NSLOT;
      trd<(slot & 3) * 16384 + ((n >> 3) & 1) * 8192 + ((n >> 1) & 1) * 4096 + ((n >> 2) & 1) * 2048 + h * 1024>(A[u & 3][h], ra[2 + (n & 1)]);
    }
  };
  auto fixA = [&](auto uc) AINL {
    constexpr int u = decltype(uc)::v;
    if constexpr (u < U1 && u >= 16 && (u & 15) < 4) {
      constexpr int g = u >> 4, a = (g % NSLOT) >> 2, p = ((g - 1) % NSLOT) >> 2;
      if constexpr (a != p) vadd_opq<(a - p) * 65536>(ra[u & 3]);
    }
    if constexpr (u == U1) { ra[0] = sb + C2OFF + rrow * 32 + (lane & 3) * 8; opq_v(ra[0]); }
    if constexpr (u == U2 || u == U2 + 1) { ra[2 + u - U2] = sb + rrow * 64 + (((u - U2) ^ ((rrow >> 2) & 1)) * 32) + (lane & 3) * 8; opq_v(ra[2 + u - U2]); }
    if constexpr (u >= U2 + 16 && u < UE && ((u - U2) & 15) < 2) {
      constexpr int g = 9 + ((u - U2) >> 4), a = (g % NSLOT) >> 2, p = ((g - 1) % NSLOT) >> 2;
      if constexpr (a != p) vadd_opq<(a - p) * 65536>(ra[2 + (u & 1)]);
    }
  };
  wait_vm<SC.cnt(0, OW, 1)>();
  abar();
  STAMP_MID(1);
  sfor<0, 3>([&](auto uc) AINL { ldA(uc, IC<0>{}); ldA(uc, IC<1>{}); });
  sfor<0, UE>([&](auto uc) AINL {
    constexpr int u = decltype(uc)::v;
    if constexpr (u == U1) { STAMP_MID(2); }
    if constexpr (u == U2) { STAMP_MID(3); }
    if constexpr (u == U3) { STAMP_MID(4); }
    if constexpr (u == Sched::uc2(4)) {
      if ((w & 3) >= 2) { w2lo -= 64 * (F_ * 2); c2dst -= 2048; }
      opq_v(w2lo); opq_s(c2dst);
    }
    if constexpr (u == Sched::uw(9, 0)) {
      int row = lane >> 2, tile = ((lane >> 1) & 1) ^ ((row >> 2) & 1);
      wlo = (row + 16 * (w & 3)) * (F_ * 2) + tile * 32 + (lane & 1) * 16 + (G * 9 + 2 * j + 1) * 128; opq_v(wlo);
    }
    wait_vl<SC.vmat(u), (u >= UE - 1 ? 0 : u == UE - 2 ? 2 : 4)>();
    if constexpr (SC.barat(u) >= 0) abar();
    if constexpr (u == 16) {
      opq_v(sq[0]); opq_v(sq[1]); opq_v(sq[2]); opq_v(sq[3]); opq_v(sq[4]);
      float s = (__uint_as_float(sq[0]) + __uint_as_float(sq[1])) + (__uint_as_float(sq[2]) + __uint_as_float(sq[3])) + (lane < 32 ? __uint_as_float(sq[4]) : 0.f);
      auto sw = __builtin_amdgcn_permlane32_swap(__float_as_uint(s), __float_as_uint(s), false, false);
      s = __uint_as_float(sw[0]) + __uint_as_float(sw[1]);
      float var = rbf(s * (1.f / D_));
      float sd = rbf(__builtin_sqrtf(rbf(var + 1e-5f)));
      auto s2 = __builtin_amdgcn_permlane16_swap(__float_as_uint(sd), __float_as_uint(sd), false, false);
      sdv[0] = __uint_as_float(s2[0]); sdv[1] = __uint_as_float(s2[1]);
      nav[0] = -1.44269504088896f * __builtin_amdgcn_rcpf(sdv[0]); nav[1] = -1.44269504088896f * __builtin_amdgcn_rcpf(sdv[1]);
      opq_v(sdv[0]); opq_v(sdv[1]); opq_v(nav[0]); opq_v(nav[1]);
    }
    constexpr int sl = u < U1 ? (u & 3) : u < U2 ? 4 : u < U3 ? (u & 1) : 2 + (u & 1);
    constexpr int m2 = u < U1 ? ((u >> 2) & 1) * 2 : u < U2 ? (u & 1) * 2 : (u & 2);
    constexpr int ks = u < U1 ? u >> 3 : u < U2 ? (u - U1) >> 1 : u < U3 ? (u - U2) >> 2 : (u - U3) >> 2;
    const u32x4 av = cat4(A[u & 3][0], A[u & 3][1]);
    if constexpr (ks == 0) mf0(acc[sl][m2], av, X[0]); else mf(acc[sl][m2], av, X[2 * ks]);
    ldA(IC<u + 3>{}, IC<0>{});
    if constexpr (u == upr(0) - 1) { pk_rd = sb + C2OFF + (w >> 2) * 4096 + rrow * 128 + (((w & 3) ^ ((rrow >> 1) & 3)) * 32) + (lane & 3) * 8; opq_v(pk_rd); }
    if constexpr (u == upr(0) || u == upr(1) || u == upr(2)) { constexpr int i = (u - upr(0)) / 16;
      u32x2 a, c; trd<i * 8192>(a, pk_rd); trd<i * 8192 + 2048>(c, pk_rd); pkv = cat4(a, c); }
    valu(IC<2 * u>{});
    vm_set(IC<2 * u + 1>{});
    vm_go(IC<2 * u>{});
    if constexpr (ks == 0) mf0(acc[sl][m2 + 1], av, X[1]); else mf(acc[sl][m2 + 1], av, X[2 * ks + 1]);
    ldA(IC<u + 3>{}, IC<1>{});
    valu(IC<2 * u + 1>{});
    vm_go(IC<2 * u + 1>{});
    fixA(IC<u + 4>{});
  });
  STAMP_MID(5);
  asm volatile("s_nop 7\n\ts_nop 7");
  sfor<VM1, VCV + 1>([&](auto opc) AINL {
    sfor<2, 4>([&](auto slc) AINL { sfor<0, (decltype(opc)::v == VCV ? 4 : 8)>([&](auto vc) AINL { vop(opc, slc, vc); }); });
  });
  sfor<0, 2>([&](auto tc) AINL { constexpr int tk = decltype(tc)::v;
    gstore16_wt<3 * 1024>(ab[tk], l16, u32x4{ov[2][2 * tk], ov[2][2 * tk + 1], ov[3][2 * tk], ov[3][2 * tk + 1]}); });
  STAMP_MID(6);
  STAMP_END(ws, 2);
}

namespace k4 {
enum { OA = 0, ORW, ORR, OADD, OCVT, OWS, OW, OX, OH, OPS, OPL, NK };
constexpr int QO = 2 * LA + 8, NQ = 2 * UE + QO + 1;
struct Sched {
  int n; short Q[640], K[640], I[640];
  short first[NQ + 1];
  short loc[NK][170];
  short cvm[641], clg[641];
  signed char vm[2 * UE + 1], lg[2 * UE + 1], bar[UE + 1];
  int n0; short q0[640], k0[640], i0[640];
  int vm0;
  constexpr void add(int q, int k, int i) { q0[n0] = q; k0[n0] = k; i0[n0] = i; ++n0; }
  constexpr int cnt(int q, int k, int i) const {
    int j = loc[k][i], f = first[q + QO];
    return k >= OW ? cvm[f] - cvm[j + 1] : clg[f] - clg[j + 1];
  }
  constexpr void need(int q, int k, int i) {
    int c = cnt(q, k, i);
    if (Q[loc[k][i]] >= q) c = -100;
    if (k >= OW) { if (c < vm[q]) vm[q] = c; } else { if (c < lg[q]) lg[q] = c; }
  }
  constexpr Sched() : n(0), Q{}, K{}, I{}, first{}, loc{}, cvm{}, clg{}, vm{}, lg{}, bar{}, n0(0), q0{}, k0{}, i0{}, vm0(0) {
    int ub[40] = {}, nb = 0;
    int sbar[2] = {UA + 3, UB + 3};
    for (int u = 5; u < UA - 4; u += 8) ub[nb++] = u;
    ub[nb] = UA - 3 > ub[nb - 1] + 2 ? UA - 3 : 0; if (ub[nb]) ++nb;
    ub[nb++] = UA + 3;
    if (UA < UB) { ub[nb++] = UA + 11; ub[nb++] = UB + 3; }
    ub[nb++] = UB + 11; ub[nb++] = UB + 19;
    for (int j = 0; j < nb; ++j) bar[ub[j]] = 1;
    for (int u = 3; u < UE; ++u) add(2 * (u - 3), OA, u);
    int qadd[7] = {}, qps[4] = {};
    for (int g = 0; g < 2; ++g) {
      int a = g ? 6 : 0, b = g ? 7 : N0, E = g ? UB : UA;
      if (g && N0 == 7) break;
      for (int s = a; s < b; ++s) add(2 * (E - (b - s)) + 4, ORW, s);
      int q = 2 * sbar[g];
      for (int s = a; s < b; ++s) add(q + s - a, ORR, s);
      q += 4;
      for (int s = a; s < b; ++s) {
        qadd[s] = q;
        for (int i = 0; i < 4; ++i) add(q + (i >> 1), OADD, 4 * s + i);
        for (int i = 0; i < 2; ++i) add(q + 2, OCVT, 2 * s + i);
        q += 3;
        if ((s & 1) || s == 6) qps[s >> 1] = q;
      }
    }
    int pw = -1000;
    for (int r = 0; r < NR; ++r) {
      int u = 4 * r - LA;
      if (4 * r >= UA) u = UA - LA + (4 * r - UA) / K4_FAST;
      if (r >= RR_) { int j = 0; while (ub[j] < 4 * (r - RR_) + 3) ++j; if (u < ub[j]) u = ub[j]; }
      if (2 * u + 1 < pw + 2) u = (pw + 1) / 2;
      pw = 2 * u + 1;
      add(pw - 1, OWS, r); add(pw, OW, r);
    }
    for (int i = 0; i < 36; ++i) add(2 * (N0 * (i >> 1) - LA + 2 * (i & 1)) + 2, OX, i);
    for (int i = 0; i < 2; ++i) add(2 * (UPL - 8) + 2 * i, OH, i);
    for (int i = 0; i < 4; ++i) add(qps[i], OPS, i);
    for (int i = 0; i < 6; ++i) add(2 * UPL + 2 * i, OPL, i);
    short cn[NQ + 1] = {};
    for (int j = 0; j < n0; ++j) ++cn[q0[j] + QO];
    int acc = 0;
    for (int q = 0; q <= NQ; ++q) { first[q] = acc; acc += cn[q]; cn[q] = first[q]; }
    for (int j = 0; j < n0; ++j) { int p = cn[q0[j] + QO]++; Q[p] = q0[j]; K[p] = k0[j]; I[p] = i0[j]; loc[k0[j]][i0[j]] = p; }
    n = n0;
    for (int j = 0; j < n; ++j) { cvm[j + 1] = cvm[j] + (K[j] >= OW); clg[j + 1] = clg[j] + (K[j] <= ORR); }
    for (int q = 0; q <= 2 * UE; ++q) { vm[q] = 63; lg[q] = 15; }
    for (int u = 3; u < UE; ++u) need(2 * u, OA, u);
    for (int k = 1; k < 18; ++k) need(2 * N0 * k, OX, 2 * k + 1);
    for (int j = 0; j < nb; ++j) { int r = j + 1 < nb ? (ub[j + 1] + 2) / 4 : NR - 1; need(2 * ub[j], OW, r); }
    need(2 * sbar[0], ORW, N0 - 1);
    if (N0 < 7) need(2 * sbar[1], ORW, 6);
    for (int s = 0; s < 7; ++s) need(qadd[s], ORR, s);
    int c1 = cnt(0, OW, (ub[0] + 2) / 4), c2 = cnt(0, OX, 1);
    vm0 = c1 < c2 ? c1 : c2;
  }
  constexpr bool ok() const { for (int q = 0; q <= 2 * UE; ++q) if (vm[q] < 0 || lg[q] < 0) return false; return Q[n - 1] < 2 * UE; }
};
constexpr Sched SC{};
static_assert(SC.ok(), "schedule");
}

__global__ __launch_bounds__(512, 2) void k4_down(char* __restrict__ ws, u16* __restrict__ out) {
  using namespace k4;
  __shared__ __attribute__((aligned(1024))) char smem[RBO + 8 * RBS];
  STAMP_BEGIN();
  const unsigned sb = (unsigned)(size_t)smem;
  const int w = __builtin_amdgcn_readfirstlane(threadIdx.x >> 6);
  const int lane = threadIdx.x & 63;
  const int b = blockIdx.x, q = b & 3, bi = b >> 3;
  const int tt = ((b >> 2) & 1) * 8 + (bi >> 2), cg = bi & 3;
  const int m = w >> 1, kh = w & 1;
  const int tb = tt * 8 + w;
  const u32x4 rsW = mkrsrc(ws + WS_WDP + (size_t)(q * 4 + cg) * (324 * 1024), 324 * 1024);
  const unsigned l16 = lane * 16;
  const unsigned wlo = l16 + w * 1024;
  const unsigned wdst = sb + w * 1024;
  const char* xb[2][3];
  for (int k = 0; k < 2; ++k) for (int i = 0; i < 3; ++i) { xb[k][i] = ws + WS_ACT + ((size_t)(tb ^ k) * 144 + q * 36 + kh * 18) * 1024 + 4096 + i * 8192; opq_s(xb[k][i]); }
  unsigned ra[2] = {sb + kh * 1024 + l16, sb + kh * 1024 + l16 + 65536};
  unsigned rwa = sb + RBO + w * RBS + l16, rra = sb + RBO + (w ^ 1) * RBS + l16;
  opq_v(ra[0]); opq_v(ra[1]); opq_v(rwa); opq_v(rra);
  char* pb = ws + WS_PART + (size_t)(b & ~3) * PART_BLK;
  char* psb[4]; const char* plb[2]; const char* hb[2];
  for (int i = 0; i < 3; ++i) { psb[i] = pb + (size_t)((q + 1 + i) & 3) * PART_BLK + w * 3072 + i * 1024; opq_s(psb[i]); }
  psb[3] = pb + (size_t)m * PART_BLK + 24576 + ((m - q - 1) & 3) * 1024 + kh * 512; opq_s(psb[3]);
  plb[0] = pb + (size_t)q * PART_BLK + w * 3072; plb[1] = pb + (size_t)q * PART_BLK + 24576 + kh * 512; opq_s(plb[0]); opq_s(plb[1]);
  hb[0] = ws + WS_HF + ((size_t)tb * 18 + (cg >> 1) * 9 + (cg & 1) * 5 + q) * 1024;
  hb[1] = ws + WS_HF + ((size_t)tb * 18 + (cg >> 1) * 9 + 4) * 1024 + (cg & 1) * 8; opq_s(hb[0]); opq_s(hb[1]);
  char* ob = (char*)out + (size_t)tb * 16 * 1152 + (cg >> 1) * 576;
  char* ob0 = ob + ((cg & 1) * 5 + q) * 64; char* ob1 = ob + 256 + (cg & 1) * 32; opq_s(ob0); opq_s(ob1);
  unsigned l8 = lane * 8, l8m = m == q ? lane * 8 : 0; opq_v(l8); opq_v(l8m);
  u32x4 X[36], A[4], P[4], sel0, sel1, selb;
  u32x2 Ps[4];
  f32x4 acc[9][2], rr[7];
  unsigned ov[7][2], oo0, oo1;
  unsigned so;
  auto op = [&](auto jc) AINL {
    constexpr int j = decltype(jc)::v, k = SC.K[j], i = SC.I[j];
    if constexpr (k == OA) { constexpr int o = (2 * i % RING) * 1024; rd128<(o & 65535)>(A[i & 3], ra[o >> 16]); }
    else if constexpr (k == ORW) wr128<i % N0 * 1024>(rwa, acc[i][1]);
    else if constexpr (k == ORR) rd128f<i % N0 * 1024>(rr[i], rra);
    else if constexpr (k == OADD) { float t = acc[i >> 2][0][i & 3]; a_add(t, rr[i >> 2][i & 3]); acc[i >> 2][0][i & 3] = t; }
    else if constexpr (k == OCVT) a_cvt(ov[i >> 1][i & 1], acc[i >> 1][0][2 * (i & 1)], acc[i >> 1][0][2 * (i & 1) + 1]);
    else if constexpr (k == OWS) dma_set<i * 8192, (8 * i % RING) * 1024>(so, wdst);
    else if constexpr (k == OW) dma_go(rsW, wlo, so);
    else if constexpr (k == OX) gload16<((i >> 1) & 7) * 1024 - 4096>(X[i], xb[i & 1][i >> 4], l16);
    else if constexpr (k == OH) { if constexpr (i == 0) gload16<0>(P[3], hb[0], l16); else gload8<0>(Ps[3], hb[1], l16); }
    else if constexpr (k == OPS) {
      if constexpr (i < 3) gstore16_wt<0>(psb[i], l16, u32x4{ov[2 * i][0], ov[2 * i][1], ov[2 * i + 1][0], ov[2 * i + 1][1]});
      else gstore8_wt<0>(psb[3], l8, u32x2{ov[6][0], ov[6][1]});
    } else if constexpr (k == OPL) {
      if constexpr (i < 3) gload16_c<i * 1024>(P[i], plb[0], l16); else gload8_c<(i - 3) * 1024>(Ps[i - 3], plb[1], l8m);
    }
  };
  sfor<0, SC.first[QO]>(op);
  wait_vm<SC.vm0>();
  abar();
  STAMP_MID(1);
  sfor<0, 3>([&](auto uc) AINL { constexpr int u = decltype(uc)::v; rd128<2048 * u>(A[u], ra[0]); });
  sfor<0, 2 * UE>([&](auto qc) AINL {
    constexpr int g = decltype(qc)::v, u = g >> 1, i = g & 1, sl = slot_of(u), ks = ks_of(u);
    if constexpr (g == 2 * UA) { STAMP_MID(2); }
    if constexpr (g == 2 * UB + 16) { STAMP_MID(3); }
    wait_vl2<SC.vm[g], (g < 6 && i == 0 ? 2 : SC.lg[g])>();
    if constexpr (i == 0 && SC.bar[u]) abar();
    if constexpr (ks == 0) mf0(acc[sl][i], A[u & 3], X[i]); else mf(acc[sl][i], A[u & 3], X[2 * ks + i]);
    sfor<SC.first[g + QO], SC.first[g + 1 + QO]>(op);
    if constexpr (g == 2 * UE - 21) {
      opq_v(l8);
      const unsigned one = ((l8 >> 5) & 3) == (l8 >> 7) ? 0x3f80u << ((l8 & 8) * 2) : 0u;
      const unsigned d0 = (l8 & 16) ? 0u : one, d1 = (l8 & 16) ? one : 0u;
      selb = u32x4{d0, d1, d0, d1}; opq_v(selb);
    }
    if constexpr (g == 2 * UE - 17) { sel0 = u32x4{selb[0], selb[1], 0u, 0u}; opq_v(sel0); }
    if constexpr (g == 2 * UE - 15) { sel1 = u32x4{0u, 0u, selb[0], selb[1]}; opq_v(sel1); }
    if constexpr (g == 2 * UE - 13) { opq_v(l8); oo1 = ((l8 >> 3) & 15) * 1152; oo0 = oo1 + ((l8 >> 7) & 1) * 32 + (l8 >> 8) * 16; oo1 += (l8 >> 7) * 8; opq_v(oo0); opq_v(oo1); }
  });
  STAMP_MID(4);
  asm volatile("s_nop 7\n\ts_nop 3");
  wr128<1024>(rwa, acc[7][1]); wr128<2048>(rwa, acc[8][1]);
  wait_vl2<63, 0>();
  abar();
  rd128f<1024>(rr[0], rra); rd128f<2048>(rr[1], rra);
  asm volatile("s_waitcnt vmcnt(0) lgkmcnt(0)" : "+v"(P[0]), "+v"(P[1]), "+v"(P[2]), "+v"(P[3]), "+v"(Ps[0]), "+v"(Ps[1]), "+v"(Ps[2]), "+v"(Ps[3]), "+v"(rr[0]), "+v"(rr[1])::"memory");
  auto missing = [&]() AINL {
    unsigned mx = umax3(umax3(P[0][0], P[0][1], P[0][2]), umax3(P[0][3], P[1][0], P[1][1]), umax3(P[1][2], P[1][3], P[2][0]));
    mx = umax3(mx, P[2][1], __builtin_elementwise_max(P[2][2], P[2][3]));
    if (m == q) mx = umax3(mx, umax3(Ps[0][0], Ps[0][1], Ps[1][0]), umax3(Ps[1][1], Ps[2][0], Ps[2][1]));
    return __builtin_amdgcn_ballot_w64(mx == 0xffffffffu) != 0;
  };
#pragma unroll 1
  for (int spin = 0; __builtin_expect(missing(), 0) && spin < (1 << 16); ++spin) {
    asm volatile("s_sleep 1\n\tglobal_load_dwordx4 %0, %6, %8 sc1\n\tglobal_load_dwordx4 %1, %6, %8 offset:1024 sc1\n\tglobal_load_dwordx4 %2, %6, %8 offset:2048 sc1\n\t"
                 "global_load_dwordx2 %3, %7, %9 sc1\n\tglobal_load_dwordx2 %4, %7, %9 offset:1024 sc1\n\tglobal_load_dwordx2 %5, %7, %9 offset:2048 sc1\n\ts_waitcnt vmcnt(0)"
                 : "+v"(P[0]), "+v"(P[1]), "+v"(P[2]), "+v"(Ps[0]), "+v"(Ps[1]), "+v"(Ps[2]) : "v"(l16), "v"(l8m), "s"(plb[0]), "s"(plb[1]) : "memory");
  }
  asm volatile("s_nop 1" : "+v"(P[0]), "+v"(P[1]), "+v"(P[2]), "+v"(Ps[0]), "+v"(Ps[1]), "+v"(Ps[2]), "+v"(Ps[3]));
  STAMP_MID(5);
  sfor<0, 4>([&](auto ic) AINL { constexpr int i = decltype(ic)::v; mf(acc[7][0], sel0, P[i]); mf(acc[8][0], sel1, P[i]); });
  if (m == q) { mf(acc[6][0], selb, cat4(Ps[0], Ps[1])); mf(acc[6][0], selb, cat4(Ps[2], Ps[3])); }
  asm volatile("s_nop 7\n\ts_nop 3" : "+v"(acc[6][0]), "+v"(acc[7][0]), "+v"(acc[8][0])::"memory");
  { const f32x4 a = acc[7][0] + rr[0], c = acc[8][0] + rr[1];
    auto s0 = __builtin_amdgcn_permlane16_swap(pk2(a[0], a[1]), pk2(c[0], c[1]), false, false);
    auto s1 = __builtin_amdgcn_permlane16_swap(pk2(a[2], a[3]), pk2(c[2], c[3]), false, false);
    gstore16_wt<0>(ob0, oo0, u32x4{s0[0], s1[0], s0[1], s1[1]});
    if (m == q) gstore8_wt<0>(ob1, oo1, u32x2{pk2(acc[6][0][0], acc[6][0][1]), pk2(acc[6][0][2], acc[6][0][3])});
  }
  STAMP_END(ws, 3);
}

static constexpr int g_mask = 15;
extern "C" void kernel_launch(void* const* d_in, const int* in_sizes, int n_in, void* d_out, int out_size, void* d_ws, size_t ws_size,
                              hipStream_t stream) {
  const u16* x = (const u16*)d_in[0];
  const u16* wqkv = (const u16*)d_in[1];
  const u16* wo = (const u16*)d_in[2];
  const u16* wg = (const u16*)d_in[4];
  const u16* wu = (const u16*)d_in[5];
  const u16* wd = (const u16*)d_in[6];
  char* ws = (char*)d_ws + ((ws_size / 2) & ~(size_t)0x1fffff);
  if (g_mask & 1) k1_attn<<<256, 512, 0, stream>>>(x, wqkv, wo, ws);
  if (g_mask & 2) k2_proj<<<234, 512, 0, stream>>>(x, wo, ws);
  if (g_mask & 4) k3_gateup<<<256, 512, 0, stream>>>(wg, wu, wd, ws);
  if (g_mask & 8) k4_down<<<256, 512, 0, stream>>>(ws, (u16*)d_out);
}
```

```cpp
#include <hip/hip_runtime.h>
#include <stdint.h>

typedef unsigned short u16;
typedef unsigned long long u64;
typedef __attribute__((ext_vector_type(8))) __bf16 bf16x8;
typedef __attribute__((ext_vector_type(2))) __bf16 bf16x2;
typedef __attribute__((ext_vector_type(4))) short s16x4;
typedef __attribute__((ext_vector_type(4))) float f32x4;
typedef __attribute__((ext_vector_type(2))) float f32x2;
typedef __attribute__((ext_vector_type(16))) float f32x16;
typedef __attribute__((ext_vector_type(4))) unsigned u32x4;
typedef __attribute__((ext_vector_type(2))) unsigned u32x2;
#define DI __device__ __forceinline__
#define LDSP(T, a) ((__attribute__((address_space(3))) T*)(a))
#define AINL __attribute__((always_inline))

template <int I> struct IC { static constexpr int v = I; };
template <int B, int E, class F> DI void sfor(F&& f) {
  if constexpr (B + 1 == E) f(IC<B>{});
  else if constexpr (B < E) { sfor<B, (B + E) / 2>(f); sfor<(B + E) / 2, E>(f); }
}

constexpr int S_ = 2048, D_ = 576, F_ = 4608;

constexpr size_t WS_OF   = 0;
constexpr size_t WS_HC   = WS_OF + (size_t)S_ * D_ * 2;
constexpr size_t WS_HF   = WS_HC + (size_t)S_ * D_ * 2;
constexpr size_t WS_ACT  = WS_HF + (size_t)S_ * D_ * 2;
constexpr size_t WS_PART = WS_ACT + (size_t)S_ * F_ * 2;
constexpr size_t PART_BLK = 28672;
constexpr size_t WS_WDP  = WS_PART + (size_t)256 * PART_BLK;
constexpr size_t WS_SSQ  = WS_WDP + (size_t)16 * 324 * 1024;
constexpr size_t WS_MEANX = WS_SSQ + (size_t)10 * S_ * 4;
constexpr size_t WS_RM   = WS_MEANX + (size_t)S_ * 4;
constexpr size_t WS_STAMP = WS_RM + 4096;

#define STAMP_BEGIN()
#define STAMP_MID(i)
#define STAMP_END(ws, k)
#define STAMP_ARG
#define STAMP_PASS

DI float bf2f(u16 v) { return __uint_as_float(((unsigned)v) << 16); }
DI float rbf(float f) { return (float)(__bf16)f; }
DI unsigned pk2(float a, float b) { bf16x2 v = {(__bf16)a, (__bf16)b}; return __builtin_bit_cast(unsigned, v); }
DI float lo16(unsigned u) { return __uint_as_float(u << 16); }
DI float hi16(unsigned u) { return __uint_as_float(u & 0xffff0000u); }

template <int N> DI void wait_vm() { asm volatile("s_waitcnt vmcnt(%0)" ::"i"(N) : "memory"); }
template <int N> DI void wait_vm_tie(u32x4& a) { asm volatile("s_waitcnt vmcnt(%1)" : "+v"(a) : "i"(N) : "memory"); }
DI void barrier() { __builtin_amdgcn_s_barrier(); }
#define SCHED() __builtin_amdgcn_sched_barrier(0)

DI void glds16(const void* sbase, unsigned voff, unsigned lds) {
  asm volatile("s_mov_b32 m0, %2\n\ts_nop 0\n\tglobal_load_lds_dwordx4 %0, %1" ::"v"(voff), "s"(sbase), "s"(lds) : "memory");
}
template <int C> DI void vadd_opq(unsigned& a) { asm volatile("v_add_u32 %0, %1, %0" : "+v"(a) : "i"(C)); }
template <int OFF> DI void gload16(u32x4& d, const void* sbase, unsigned voff) {
  asm volatile("global_load_dwordx4 %0, %1, %2 offset:%3" : "=v"(d) : "v"(voff), "s"(sbase), "i"(OFF) : "memory");
}
template <int OFF> DI void gload4(unsigned& d, const void* sbase, unsigned voff) {
  asm volatile("global_load_dword %0, %1, %2 offset:%3" : "=v"(d) : "v"(voff), "s"(sbase), "i"(OFF) : "memory");
}
template <int OFF> DI void gstore16_wt(void* sbase, unsigned voff, u32x4 v) {
  asm volatile("global_store_dwordx4 %0, %1, %2 offset:%3 sc1\n\ts_nop 1" ::"v"(voff), "v"(v), "s"(sbase), "i"(OFF) : "memory");
}
#define ACT_POL "sc1"
template <int OFF> DI void gstore16_act(void* sbase, unsigned voff, u32x4 v) {
  asm volatile("global_store_dwordx4 %0, %1, %2 offset:%3 " ACT_POL "\n\ts_nop 1" ::"v"(voff), "v"(v), "s"(sbase), "i"(OFF) : "memory");
}
DI s16x4 trread(unsigned a) { return __builtin_amdgcn_ds_read_tr16_b64_v4i16(LDSP(s16x4, a)); }
DI bf16x8 cat8(s16x4 a, s16x4 b) { return __builtin_bit_cast(bf16x8, __builtin_shufflevector(a, b, 0, 1, 2, 3, 4, 5, 6, 7)); }
DI f32x16 mfma32(bf16x8 a, u32x4 b, f32x16 c) { return __builtin_amdgcn_mfma_f32_32x32x16_bf16(a, __builtin_bit_cast(bf16x8, b), c, 0, 0, 0); }
DI f32x4 mfma16(bf16x8 a, u32x4 b, f32x4 c) { return __builtin_amdgcn_mfma_f32_16x16x32_bf16(a, __builtin_bit_cast(bf16x8, b), c, 0, 0, 0); }

DI void mf(f32x4& c, const u32x4& a, const u32x4& b) { asm volatile("v_mfma_f32_16x16x32_bf16 %0, %1, %2, %0" : "+v"(c) : "v"(a), "v"(b)); }
DI void mf0(f32x4& c, const u32x4& a, const u32x4& b) { asm volatile("v_mfma_f32_16x16x32_bf16 %0, %1, %2, 0" : "=v"(c) : "v"(a), "v"(b)); }
template <int OFF> DI void trd(u32x2& d, unsigned a) { asm volatile("ds_read_b64_tr_b16 %0, %1 offset:%2" : "=v"(d) : "v"(a), "i"(OFF)); }
template <int OFF> DI void rd128(u32x4& d, unsigned a) { asm volatile("ds_read_b128 %0, %1 offset:%2" : "=v"(d) : "v"(a), "i"(OFF)); }
template <int V, int L> DI void wait_vl() {
  if constexpr (V >= 63) asm volatile("s_waitcnt lgkmcnt(%0)" ::"i"(L) : "memory");
  else asm volatile("s_waitcnt vmcnt(%0) lgkmcnt(%1)" ::"i"(V), "i"(L) : "memory");
}
DI void abar() { asm volatile("s_barrier" ::: "memory"); }
DI u32x4 mkrsrc(const void* p, unsigned bytes) { u32x4 r = {(unsigned)(size_t)p, (unsigned)((size_t)p >> 32) & 0xffffu, bytes, 0x20000u}; return r; }
template <int SO, int LO> DI void dma_set(unsigned& so, unsigned ldsbase) {
  asm volatile("s_add_u32 m0, %1, %3\n\ts_mov_b32 %0, %2" : "=s"(so) : "s"(ldsbase), "i"(SO), "i"(LO) : "scc");
}
DI void dma_setr(unsigned lds) { asm volatile("s_mov_b32 m0, %0" ::"s"(lds)); }
DI void dma_go(const u32x4& rs, unsigned voff, unsigned so) {
  asm volatile("buffer_load_dwordx4 %0, %1, %2 offen lds" ::"v"(voff), "s"(rs), "s"(so) : "memory");
}
template <class T> DI void opq_s(T& v) { asm volatile("" : "+s"(v)); }
template <class T> DI void opq_v(T& v) { asm volatile("" : "+v"(v)); }
DI u32x4 cat4(const u32x2& a, const u32x2& b) { return u32x4{a[0], a[1], b[0], b[1]}; }

DI void a_mul(float& d, float a, float b) { asm volatile("v_mul_f32 %0, %1, %2" : "=v"(d) : "v"(a), "v"(b)); }
DI void a_exp(float& d) { asm volatile("v_exp_f32 %0, %0" : "+v"(d)); }
DI void a_rcp(float& d) { asm volatile("v_rcp_f32 %0, %0" : "+v"(d)); }
DI void a_fma11(float& d, float s) { asm volatile("v_fma_f32 %0, %0, %1, %1" : "+v"(d) : "v"(s)); }
DI void a_cvt(unsigned& d, float a, float b) { asm volatile("v_cvt_pk_bf16_f32 %0, %1, %2" : "=v"(d) : "v"(a), "v"(b)); }
DI void a_pl16(unsigned& a, unsigned& b) { asm volatile("v_permlane16_swap_b32 %0, %1" : "+v"(a), "+v"(b)); }

template <int OFF> DI void wr128(unsigned a, const f32x4& v) { asm volatile("ds_write_b128 %0, %1 offset:%2" ::"v"(a), "v"(v), "i"(OFF) : "memory"); }
template <int OFF> DI void rd128f(f32x4& d, unsigned a) { asm volatile("ds_read_b128 %0, %1 offset:%2" : "=v"(d) : "v"(a), "i"(OFF)); }
DI void a_add(float& d, float a) { asm volatile("v_add_f32 %0, %0, %1" : "+v"(d) : "v"(a)); }
template <int OFF> DI void gload16_c(u32x4& d, const void* sbase, unsigned voff) {
  asm volatile("global_load_dwordx4 %0, %1, %2 offset:%3 sc1" : "=v"(d) : "v"(voff), "s"(sbase), "i"(OFF) : "memory");
}
template <int OFF> DI void gload8_c(u32x2& d, const void* sbase, unsigned voff) {
  asm volatile("global_load_dwordx2 %0, %1, %2 offset:%3 sc1" : "=v"(d) : "v"(voff), "s"(sbase), "i"(OFF) : "memory");
}
template <int OFF> DI void gload8(u32x2& d, const void* sbase, unsigned voff) {
  asm volatile("global_load_dwordx2 %0, %1, %2 offset:%3" : "=v"(d) : "v"(voff), "s"(sbase), "i"(OFF) : "memory");
}
template <int OFF> DI void gstore8_wt(void* sbase, unsigned voff, u32x2 v) {
  asm volatile("global_store_dwordx2 %0, %1, %2 offset:%3 sc1\n\ts_nop 1" ::"v"(voff), "v"(v), "s"(sbase), "i"(OFF) : "memory");
}
template <int V, int L> DI void wait_vl2() {
  if constexpr (V >= 63 && L >= 15) {}
  else if constexpr (V >= 63) asm volatile("s_waitcnt lgkmcnt(%0)" ::"i"(L) : "memory");
  else if constexpr (L >= 15) asm volatile("s_waitcnt vmcnt(%0)" ::"i"(V) : "memory");
  else asm volatile("s_waitcnt vmcnt(%0) lgkmcnt(%1)" ::"i"(V), "i"(L) : "memory");
}
DI unsigned umax3(unsigned a, unsigned b, unsigned c) { return __builtin_elementwise_max(__builtin_elementwise_max(a, b), c); }

namespace k1 {
#define K1_I0 2
#define K1_I1 3
#define K1_LAG 3
constexpr int NG = 6, GB = 18432, KO = NG * GB, VO = KO + 80 * 400, RO = VO + 80 * 144 + 64, LDSZ = RO + 576, PO = 2 * GB;
constexpr int mn(int a, int b) { return a < b ? a : b; }
constexpr int upto(int j) { return j < 0 ? K1_I1 : mn(14, mn(j >= 1 ? j - 1 + NG : NG, j + 1 + K1_LAG)); }
constexpr int ni(int g) { return g < 14 ? 9 * g : 9 * 13 + 5; }
}
DI float dot2(unsigned a, unsigned b, float c) { return __builtin_amdgcn_fdot2_f32_bf16(__builtin_bit_cast(bf16x2, a), __builtin_bit_cast(bf16x2, b), c, false); }
DI float xsum4(float v) {
  auto r = __builtin_amdgcn_permlane32_swap(__float_as_uint(v), __float_as_uint(v), false, false); v = __uint_as_float(r[0]) + __uint_as_float(r[1]);
  auto q = __builtin_amdgcn_permlane16_swap(__float_as_uint(v), __float_as_uint(v), false, false); return __uint_as_float(q[0]) + __uint_as_float(q[1]);
}
template <int C> DI float dpp_add(float v) { return v + __builtin_bit_cast(float, __builtin_amdgcn_update_dpp(0, __builtin_bit_cast(int, v), C, 0xf, 0xf, false)); }
DI float sum8(float v) { return dpp_add<0x141>(dpp_add<0x4e>(dpp_add<0xb1>(v))); }
DI float sum32(float v) { v = dpp_add<0x140>(sum8(v)); auto q = __builtin_amdgcn_permlane16_swap(__float_as_uint(v), __float_as_uint(v), false, false); return __uint_as_float(q[0]) + __uint_as_float(q[1]); }
template <int OFF> DI void wr128u(unsigned a, const u32x4& v) { asm volatile("ds_write_b128 %0, %1 offset:%2" ::"v"(a), "v"(v), "i"(OFF) : "memory"); }
template <int H> DI void k1_logits(float (&lg)[8], unsigned sb, int a, int lane, int tt) {
  const unsigned ka = sb + k1::KO + (lane + 2) * 400 + a * 48;
  const u32x4 q0 = *LDSP(u32x4, ka + 14 * 400 + 192), q1 = *LDSP(u32x4, ka + 14 * 400 + 208); const unsigned q2 = *LDSP(unsigned, ka + 14 * 400 + 224);
  for (int i = 4 * H; i < 4 * H + 4; ++i) {
    const u32x4 k0 = *LDSP(u32x4, ka + (7 - i) * 800), k1 = *LDSP(u32x4, ka + (7 - i) * 800 + 16); const unsigned k2 = *LDSP(unsigned, ka + (7 - i) * 800 + 32);
    float d = dot2(q2, k2, 0.f);
    for (int e = 0; e < 4; ++e) { d = dot2(q0[e], k0[e], d); d = dot2(q1[e], k1[e], d); }
    lg[i] = tt * 64 + lane - 2 * i < 0 ? -1e9f : d * 0.23570226039551584f; opq_v(lg[i]);
  }
}
DI void k1_softmax(float (&lg)[8], unsigned sb, int a, int lane) {
  for (int i = 0; i < 8; ++i) opq_v(lg[i]);
  const unsigned pa = sb + k1::PO + (a * 64 + lane) * 80;
  for (int i = 0; i < 4; ++i) *LDSP(volatile u32x4, pa + 16 * i) = u32x4{0u, 0u, 0u, 0u};
  float mx = lg[0];
  for (int i = 1; i < 8; ++i) mx = __builtin_fmaxf(mx, lg[i]);
  float sm = 0.f;
  for (int i = 0; i < 8; ++i) { lg[i] = __builtin_amdgcn_exp2f((lg[i] - mx) * 1.44269504088896f); sm += lg[i]; }
  const float rs = __builtin_amdgcn_rcpf(sm);
  for (int i = 0; i < 8; ++i) *LDSP(volatile u16, pa + (lane & 1) * 32 + (lane & 14) + 16 - 2 * i) = (u16)pk2(lg[i] * rs, 0.f);
}

struct K1Stage {
  u32x4 S[12];
  float mean[2], nmb[2], ssq[2];
  unsigned wa[2], lo, hi;
  template <int I> DI void centre() { constexpr int c = I >> 1, h = (c & 3) == 3, i = (I & 1) * 2;
    unsigned v0 = S[c][i], v1 = S[c][i + 1]; float t0, t1, t2, t3;
    asm volatile("v_dot2_f32_bf16 %3, %0, %7, %9\n\tv_dot2_f32_bf16 %4, %0, %8, %9\n\tv_dot2_f32_bf16 %5, %1, %7, %9\n\tv_dot2_f32_bf16 %6, %1, %8, %9\n\ts_nop 0\n\t"
                 "v_cvt_pk_bf16_f32 %0, %3, %4\n\tv_dot2c_f32_bf16 %2, %0, %0\n\tv_cvt_pk_bf16_f32 %1, %5, %6\n\tv_dot2c_f32_bf16 %2, %1, %1"
                 : "+v"(v0), "+v"(v1), "+v"(ssq[h]), "=&v"(t0), "=&v"(t1), "=&v"(t2), "=&v"(t3) : "s"(lo), "s"(hi), "v"(nmb[h]));
    S[c][i] = v0; S[c][i + 1] = v1; }
  template <int P> DI void put() { wr128u<0>(wa[0], S[4 * P]); wr128u<128>(wa[0], S[4 * P + 1]); wr128u<256>(wa[0], S[4 * P + 2]); wr128u<0>(wa[1], S[4 * P + 3]); }
  DI void fin(unsigned sb, int w, int lane) {
    float r[2];
    asm volatile("s_nop 2" : "+v"(ssq[0]), "+v"(ssq[1]));
    for (int h = 0; h < 2; ++h) {
      const float d = mean[h] + nmb[h], var = rbf(__builtin_fmaxf((h ? sum32((lane & 31) < 24 ? ssq[1] : 0.f) : sum8(ssq[0])) * (1.f / D_) - d * d, 0.f));
      r[h] = __builtin_amdgcn_rcpf(rbf(__builtin_sqrtf(rbf(var + 1e-5f))));
    }
    *LDSP(float, sb + k1::RO + ((lane & 1) ? 2 * w + (lane >> 5) : 16 + 8 * w + (lane >> 3)) * 4) = (lane & 1) ? r[1] : r[0];
  }
};

#define PF1 0
#define PF2 9
#define PF_AT (S1 + 1)
DI void pf_mlp(int i, const u16* Wg, const u16* Wu, const u16* Wd, int lane) {
  const int b = blockIdx.x, bi = b >> 3;
  const unsigned id = min(lane, 53), r = (id * 3641) >> 16;
  const char* base = i < 6 ? (const char*)((i & 1) ? Wu : Wg) + (size_t)(((i >> 1) * 64 + bi * 2 + ((b >> 2) & 1)) * 3) * (F_ * 2) + (b & 3) * 2304 : (const char*)Wd + (size_t)(b * 3 + i - 6) * 6912;
  asm volatile("global_load_lds_dword %0, %1" ::"v"((i < 6 ? r * (F_ * 2 - 2304) : 0) + id * 128), "s"(base) : "memory");
}
#define PFC 5
#define PF_G0 1
#define PFQ 0

template <int R> DI void k1_gemm(K1Stage& st, const u16* __restrict__ Wo, const u16* Wg, const u16* Wu, const u16* Wd, char* __restrict__ ws, unsigned sb, int w, int lane, int tt STAMP_ARG) {
  using namespace k1;
  constexpr int NB = R < 2 ? 2 : 1, N1 = R == 0 ? 5 : R == 1 ? 4 : 3, N2 = R == 0 ? 2 : 3, T1 = R == 1 ? 5 : 0, T2 = R == 1 ? 2 : 0;
  constexpr int S1 = 18 * N1, NS = S1 + 18 * N2, PD = 3;
  const int a = w & 1;
  const int lrow = (R < 2 ? 16 + 32 * a : 0) + (lane & 15);
  u32x4 X[NB][18];
  const int t0 = R < 2 ? 0 : 2 * a;
  unsigned ra[2], xa = sb + KO + lrow * 416 + (lane >> 4) * 16;
  ra[0] = sb + ((lane >> 4) * 4 + ((lane & 15) >> 2)) * 288 + (lane & 3) * 8 + t0 * 32; ra[1] = ra[0] + 3 * GB;
  opq_v(ra[0]); opq_v(ra[1]); opq_v(xa);
  float rstd[NB];
  u32x2 rmv[9];
  const int rmr = 8 * blockIdx.x + 4 * a + (lane >> 4);
  if constexpr (R == 2) { const unsigned o = rmr < D_ ? rmr * (D_ * 2) + (lane & 15) * 8 : 0;
    sfor<0, 9>([&](auto ic) AINL { constexpr int i = decltype(ic)::v; gload8<i * 128>(rmv[i], Wo, o); }); }
  f32x4 acc[N1][NB], acv[N2][NB];
  u32x2 A[PD + 1][2];
  float lg[8];
  sfor<0, NS + PD>([&](auto sc) AINL {
    constexpr int s = decltype(sc)::v;
    if constexpr (s < NS) {
      constexpr int ks = s < S1 ? s / N1 : (s - S1) / N2, j = s < S1 ? s % N1 : (s - S1) % N2, g = s < S1 ? ks >> 1 : 9 + (ks >> 2);
      if constexpr (j == 0 && (s < S1 ? ks & 1 : ks & 3) == 0) {
        if constexpr (g == 0 || g == 6 || g == 10) asm volatile("s_waitcnt lgkmcnt(0)" ::: "memory");
        abar();
        if constexpr (g == 0) { STAMP_MID(2); }
        if constexpr (g == 9) { STAMP_MID(3); }
        if constexpr (g == 13) { STAMP_MID(4); }
        if constexpr (g == 1 || g == 3) { SCHED(); st.template put<(g + 1) / 2>(); }
        if constexpr (g == 4) st.fin(sb, w, lane);
        if constexpr (R == 1 && g >= PF_G0 && g < PF_G0 + PF1) { if (a == 0) { dma_setr(sb + RO + 320); pf_mlp(g - PF_G0, Wg, Wu, Wd, lane); } }
      }
      if constexpr (g <= 4 && g % 2 == 0 && s - 2 * N1 * g < 3)
        sfor<0, 2 * NB>([&](auto ic) AINL { constexpr int k = 2 * (s - 2 * N1 * g) + decltype(ic)::v / NB, n = decltype(ic)::v % NB; rd128<n * 16 * 416 + k * 64>(X[n][3 * g + k], xa); });
      constexpr int off = (g % 3) * GB + (s < S1 ? (ks & 1) * 9216 + T1 * 32 : ((ks >> 1) & 1) * 9216 + (ks & 1) * 144 + T2 * 32) + j * 32;
      trd<off>(A[s % (PD + 1)][0], ra[g % NG / 3]); trd<off + 4608>(A[s % (PD + 1)][1], ra[g % NG / 3]);
      if constexpr (s < PD) st.template centre<8 + s>();
    }
    if constexpr (s >= PD) {
      constexpr int m = s - PD, ks = m < S1 ? m / N1 : (m - S1) / N2, j = m < S1 ? m % N1 : (m - S1) % N2;
      wait_vl2<63, 2 * (s < NS ? PD : NS - 1 - m)>();
      const u32x4 av = cat4(A[m % (PD + 1)][0], A[m % (PD + 1)][1]);
      sfor<0, NB>([&](auto bc) AINL { constexpr int n = decltype(bc)::v;
        if constexpr (m < S1) { if constexpr (ks == 0) mf0(acc[j][n], av, X[n][0]); else mf(acc[j][n], av, X[n][ks]); }
        else { if constexpr (ks == 0) mf0(acv[j][n], av, X[n][0]); else mf(acv[j][n], av, X[n][ks]); }
        });
      constexpr int c0 = 11 + (R < 2 ? m : m < 3 ? 2 * m : m + 3), c1 = mn(24, c0 + (R == 2 && m < 3 ? 2 : 1));
      sfor<c0, c1>([&](auto ic) AINL { st.template centre<decltype(ic)::v>(); });
      if constexpr (R == 2 && m == 10 * N1) {
        asm volatile("s_waitcnt vmcnt(0)" : "+v"(rmv[0]), "+v"(rmv[1]), "+v"(rmv[2]), "+v"(rmv[3]), "+v"(rmv[4]), "+v"(rmv[5]), "+v"(rmv[6]), "+v"(rmv[7]), "+v"(rmv[8])::"memory");
        float t = 0.f;
        for (int i = 0; i < 9; ++i) { t = dot2(rmv[i][0], 0x3f803f80u, t); t = dot2(rmv[i][1], 0x3f803f80u, t); }
        t = dpp_add<0x140>(sum8(t)) * (1.f / D_);
        const float hi = rbf(t);
        const unsigned v = pk2((lane & 1) ? t - hi : hi, 0.f);
        if ((lane & 15) < 2 && rmr < D_) asm volatile("global_store_short %0, %1, %2 sc1" ::"v"((lane & 1) * (D_ * 2) + rmr * 2), "v"(v), "s"(ws + WS_RM) : "memory");
      }
      if constexpr (m == S1 - 8) for (int n = 0; n < NB; ++n) { rstd[n] = *LDSP(float, sb + RO + (lrow + 16 * n) * 4); opq_v(rstd[n]); }
      if constexpr (m >= S1 && m < S1 + N1) {
        constexpr int t = m - S1;
        for (int n = 0; n < NB; ++n) {
          opq_v(acc[t][n]);
          for (int e = 0; e < 2; ++e) { const unsigned p = 8 * (t0 + T1 + t) + 2 * (lane >> 4) + e;
            *LDSP(unsigned, sb + KO + (lrow + 16 * n) * 400 + 4 * p + 12 * ((p * 57) >> 9)) = pk2(acc[t][n][2 * e] * rstd[n], acc[t][n][2 * e + 1] * rstd[n]); }
        }
      }
    }
    if constexpr (R == 2 && s == S1 + 4 * N2) k1_logits<0>(lg, sb, a, lane, tt);
    if constexpr (R == 2 && s == S1 + 8 * N2) k1_logits<1>(lg, sb, a, lane, tt);
    if constexpr (R == 2 && s == S1 + 12 * N2) k1_softmax(lg, sb, a, lane);
  });
  static_assert(N1 - 1 + PD < 4 * N2, "k, q are written before barrier 10");
  asm volatile("s_nop 7\n\ts_nop 3");
  sfor<0, N2 * NB>([&](auto jc) AINL { constexpr int t = decltype(jc)::v / NB, n = decltype(jc)::v % NB;
    opq_v(acv[t][n]);
    if (R == 0 || t < 2 || lane < 32 || (R == 2 && a == 0))
      *LDSP(u32x2, sb + VO + (lrow + 16 * n) * 144 + (t0 + T2 + t) * 32 + (lane >> 4) * 8) =
        u32x2{pk2(acv[t][n][0] * rstd[n], acv[t][n][1] * rstd[n]), pk2(acv[t][n][2] * rstd[n], acv[t][n][3] * rstd[n])}; });
}

template <int VB> DI void k1_stage0(K1Stage& st, char* __restrict__ ws, unsigned sb, int w, int lane, int tt, int hg) {
  float sum[3] = {0.f, 0.f, 0.f};
  unsigned on = (lane & 31) < 24 ? 0x3f803f80u : 0u;
  st.lo = 0x3f80u; st.hi = 0x3f800000u; opq_s(st.lo); opq_s(st.hi);
  st.wa[0] = sb + k1::KO + (16 + 8 * w + (lane >> 3)) * 416 + (lane & 7) * 16; st.wa[1] = sb + k1::KO + (2 * w + (lane >> 5)) * 416 + min(lane & 31, 23) * 16;
  sfor<0, 12>([&](auto kc) AINL { constexpr int k = decltype(kc)::v;
    if constexpr (k % 2 == 0) asm volatile("s_waitcnt vmcnt(%5)" : "+v"(st.S[k]), "+v"(st.S[k + 1]), "+v"(sum[0]), "+v"(sum[1]), "+v"(sum[2]) : "i"(VB + 10 - k) : "memory");
    for (int i = 0; i < 4; ++i) { if constexpr ((k & 3) == 3) sum[2] = dot2(st.S[k][i], on, sum[2]); else sum[i & 1] = dot2(st.S[k][i], 0x3f803f80u, sum[i & 1]); } });
  st.mean[0] = sum8(sum[0] + sum[1]) * (1.f / D_); st.mean[1] = sum32(sum[2]) * (1.f / D_);
  for (int h = 0; h < 2; ++h) { st.nmb[h] = -rbf(st.mean[h]); st.ssq[h] = 0.f; }
  sfor<0, 8>([&](auto ic) AINL { st.template centre<decltype(ic)::v>(); });
  st.template put<0>();
  if (VB <= 1 && hg == 0 && (lane & 7) == 0) asm volatile("global_store_dword %0, %1, %2 sc1" ::"v"(lane >> 1), "v"(st.mean[0]), "s"(ws + WS_MEANX + (tt * 8 + w) * 32) : "memory");
}

__global__ __launch_bounds__(512, 2) void k1_attn(const u16* __restrict__ x, const u16* __restrict__ Wq, const u16* __restrict__ Wo, const u16* Wg, const u16* Wu, const u16* Wd, char* __restrict__ ws) {
  using namespace k1;
  __shared__ __attribute__((aligned(1024))) char smem[LDSZ];
  STAMP_BEGIN();
  const unsigned sb = (unsigned)(size_t)smem;
  const int w = __builtin_amdgcn_readfirstlane(threadIdx.x >> 6);
  const int lane = threadIdx.x & 63;
  const int b = blockIdx.x, bi = b >> 3;
  const int hg = (b & 1) * 4 + (bi & 3), tt = ((b >> 1) & 3) * 8 + (bi >> 2);
  K1Stage st;
  auto ldx = [&]() AINL {
    const unsigned o0 = (tt * 64 + 8 * w + (lane >> 3)) * (D_ * 2) + (lane & 7) * 16, o1 = max(tt * 64 - 16 + 2 * w + (lane >> 5), 0) * (D_ * 2) + min(lane & 31, 23) * 16;
    sfor<0, 12>([&](auto kc) AINL { constexpr int k = decltype(kc)::v;
      if constexpr ((k & 3) == 3) gload16<(k >> 2) * 384>(st.S[k], x, o1); else gload16<(k >> 2) * 384 + (k & 3) * 128>(st.S[k], x, o0); }); };
  auto pfq = [&]() AINL {
    if constexpr (PFQ) { dma_setr(sb + RO + 320);
      const unsigned id = min(w * 64 + lane, 269), r = (id * 4370) >> 16, c = id - 15 * r, s3 = (c * 13) >> 6;
      asm volatile("global_load_lds_dword %0, %1" ::"v"((bi * 18 + r) * 3456 + s3 * 512 + c * 128 + (b & 1) * 512), "s"(Wq) : "memory"); } };
  if (w == PFC) {
    u64 pc; unsigned len;
    asm volatile("s_getpc_b64 %0\n.Lpf_here:\n\ts_mov_b32 %1, (k_text1-.Lpf_here)&4294967295" : "=s"(pc), "=s"(len));
    dma_setr(sb + RO + 320);
    asm volatile("global_load_lds_dword %0, %1" ::"v"(min((unsigned)(bi + 32 * lane) * 128u, len - 4)), "s"(pc) : "memory");
  }
  if (w < 2) { ldx(); pfq(); k1_stage0<PFQ>(st, ws, sb, w, lane, tt, hg); STAMP_MID(1); k1_gemm<0>(st, Wo, Wg, Wu, Wd, ws, sb, w, lane, tt STAMP_PASS); }
  else if (w < 4) { ldx(); pfq(); k1_stage0<PFQ>(st, ws, sb, w, lane, tt, hg); STAMP_MID(1); k1_gemm<1>(st, Wo, Wg, Wu, Wd, ws, sb, w, lane, tt STAMP_PASS); }
  else if (w < 6) { ldx(); pfq(); k1_stage0<PFQ>(st, ws, sb, w, lane, tt, hg); STAMP_MID(1); k1_gemm<2>(st, Wo, Wg, Wu, Wd, ws, sb, w, lane, tt STAMP_PASS); }
  else {
    ldx();
    const u32x4 rsW = mkrsrc(Wq, D_ * 3456);
    unsigned vo[9], so, dst = sb + (w & 1) * 1024;
    auto issue = [&](auto gc) AINL {
      constexpr int g = decltype(gc)::v;
      sfor<0, (g < 13 ? 9 : 5)>([&](auto mc) AINL { constexpr int m = decltype(mc)::v;
        if constexpr (g == 0 || g == 9) {
          const unsigned id = 128 * m + 64 * (w & 1) + lane, P = (id * 3641) >> 16, c = id - 18 * P, kr = 8 * ((P >> 2) & 3) + 4 * ((P >> 4) & 1) + (P & 3);
          if constexpr (g == 0) vo[m] = (kr + (P & 32)) * 3456 + 16 * c + (c < 9 ? 1152 : -144) + 144 * hg;
          else vo[m] = (kr + 2 * (P & 32)) * 3456 + 16 * c + (c < 9 ? 2304 : 32 * 3456 + 2160) + 144 * hg;
          opq_v(vo[m]);
        }
        dma_set<(g < 9 ? g * 64 : (g - 9) * 128) * 3456, (g % NG) * GB + m * 2048>(so, dst); dma_go(rsW, vo[m], so); });
    };
    sfor<0, K1_I0>(issue);
    k1_stage0<9 * K1_I0>(st, ws, sb, w, lane, tt, hg);
    sfor<K1_I0, upto(-1)>(issue);
    float lg[8];
    sfor<0, 14>([&](auto gc) AINL { constexpr int g = decltype(gc)::v;
      wait_vl2<ni(upto(g - 1)) - ni(g + 1), 0>();
      abar();
      sfor<upto(g - 1), upto(g)>(issue);
      if constexpr (g == 0 || g == 2) sfor<8 + 4 * g, 16 + 4 * g>([&](auto ic) AINL { st.template centre<decltype(ic)::v>(); });
      if constexpr (g == 1 || g == 3) st.template put<(g + 1) / 2>();
      if constexpr (g == 4) st.fin(sb, w, lane);
      if constexpr (g == 10) k1_logits<0>(lg, sb, w & 3, lane, tt);
      if constexpr (g == 11) k1_logits<1>(lg, sb, w & 3, lane, tt);
      if constexpr (g == 12) k1_softmax(lg, sb, w & 3, lane);
    });
    if (hg == 0 && (lane & 7) == 0) asm volatile("global_store_dword %0, %1, %2 sc1" ::"v"(lane >> 1), "v"(st.mean[0]), "s"(ws + WS_MEANX + (tt * 8 + w) * 32) : "memory");
  }
  const int hf = w & 1, g = lane >> 4;
  unsigned va = sb + VO + ((w >> 1) * 16 + (g & 1) * 16 + ((lane >> 1) & 6) + (g >> 1)) * 144 + (lane & 3) * 8 + hf * 64;
  unsigned pa = sb + PO + ((w >> 1) * 16 + (lane & 15)) * 80 + g * 16, pa1 = pa + hf * 5120, va2 = va + (hf ? 32 : 128);
  char* ob = ws + WS_OF + (size_t)(tt * 4 + (w >> 1)) * 18432 + hg * 2304;
  opq_v(va); opq_v(pa); opq_v(pa1); opq_v(va2); opq_s(ob);
  asm volatile("s_waitcnt lgkmcnt(0)" ::: "memory");
  abar();
  STAMP_MID(5);
  u32x2 V[3][2]; u32x4 P[3]; f32x4 o[5];
  rd128<0>(P[0], pa1); trd<0>(V[0][0], va); trd<1152>(V[0][1], va); rd128<5120>(P[1], pa1); trd<32>(V[1][0], va); trd<1184>(V[1][1], va);
  rd128<15360>(P[2], pa); trd<0>(V[2][0], va2); trd<1152>(V[2][1], va2);
  wait_vl2<63, 6>(); mf0(o[0], cat4(V[0][0], V[0][1]), P[0]);
  wait_vl2<63, 5>(); mf0(o[1], cat4(V[0][0], V[0][1]), P[1]);
  wait_vl2<63, 3>(); mf0(o[2], cat4(V[1][0], V[1][1]), P[0]); mf0(o[3], cat4(V[1][0], V[1][1]), P[1]);
  wait_vl2<63, 0>(); mf0(o[4], cat4(V[2][0], V[2][1]), P[2]);
  asm volatile("s_nop 7\n\ts_nop 3" : "+v"(o[0]), "+v"(o[1]), "+v"(o[2]), "+v"(o[3]), "+v"(o[4]));
  unsigned q[5][2];
  for (int t = 0; t < 5; ++t) for (int e = 0; e < 2; ++e) q[t][e] = pk2(o[t][2 * e], o[t][2 * e + 1]);
  u32x4 ov;
  for (int e = 0; e < 2; ++e) {
    const unsigned c0 = hf && g > 0 ? q[1][e] : q[0][e], c1 = hf ? (2 * g + e < 3 ? q[3][e] : q[4][e]) : (2 * g + e < 1 ? q[2][e] : q[3][e]);
    auto r = __builtin_amdgcn_permlane16_swap(c0, c1, false, false); ov[e] = r[0]; ov[2 + e] = r[1];
  }
  gstore16_wt<0>(ob, (hf * 4 + (g & 1) * 2 + (g >> 1)) * 256 + (lane & 15) * 16, ov);
  if (!hf && lane < 32) gstore8_wt<2048>(ob, (lane & 15) * 16 + g * 8, u32x2{q[4][0], q[4][1]});
  STAMP_END(ws, 0);
}

__global__ __launch_bounds__(512, 2) void k2_proj(const u16* __restrict__ x, const u16* __restrict__ Wo, const u16* Wg, const u16* Wu, const u16* Wd, char* __restrict__ ws) {
  __shared__ __attribute__((aligned(1024))) char smem[73728 + 3072 + 256];
  STAMP_BEGIN();
  const unsigned sb = (unsigned)(size_t)smem;
  const int w = __builtin_amdgcn_readfirstlane(threadIdx.x >> 6);
  const int lane = threadIdx.x & 63;
  const int b = blockIdx.x;
  const int lin = (b & 7) * 29 + (b >> 3) + ((b & 7) < 2 ? (b & 7) : 2);
  const int tt = lin / 9, cg = lin - tt * 9;
  constexpr int NPF = (PF2 - PF1 + 2) / 3;
  auto pf = [&]() AINL { dma_setr(sb + 76800); for (int i = 0; i < NPF; ++i) pf_mlp(min(PF1 + 3 * i + w - 5, PF2 - 1), Wg, Wu, Wd, lane); };
  if (b >= 234) { if (w >= 5) pf(); return; }
  if (w >= 5) {
    const u32x4 rsW = mkrsrc(Wo, D_ * D_ * 2);
    unsigned vo[4];
    for (int i = 0; i < 4; ++i) { const int p = i * 8 + (lane >> 3), k = 8 * ((p >> 2) & 3) + 4 * (p >> 4) + (p & 3);
      vo[i] = (k + 32 * (w - 5)) * (D_ * 2) + ((((lane >> 1) & 3) ^ ((p >> 1) & 3)) * 32) + (lane & 1) * 16 + cg * 128; }
    unsigned dst = sb + (w - 5) * 4096, so;
    { const u32x4 rsR = mkrsrc(ws + WS_RM, 3072); dma_set<0, 73728>(so, sb + (w - 5) * 1024); dma_go(rsR, lane * 16 + (w - 5) * 1024, so); }
    sfor<0, 24>([&](auto ic) AINL { constexpr int i = decltype(ic)::v;
      dma_set<(i >> 2) * 96 * (D_ * 2), (i >> 2) * 12288 + (i & 3) * 1024>(so, dst); dma_go(rsW, vo[i & 3], so); });
    pf();
    sfor<0, 6>([&](auto sc) AINL { wait_vm<20 + NPF - 4 * decltype(sc)::v>(); abar(); });
    return;
  }
  const int tb = min(tt * 5 + w, 127);
  u32x4 X[18], xr[2];
  { const char* ob = ws + WS_OF + (size_t)tb * 18432 + 4096;
    sfor<0, 18>([&](auto kc) AINL { constexpr int k = decltype(kc)::v; gload16<(k & 7) * 1024 - 4096>(X[k], ob + (k >> 3) * 8192, lane * 16); }); }
  { const char* xb = (const char*)x + (size_t)tb * 16 * (D_ * 2) + cg * 128; const unsigned o = (lane & 15) * (D_ * 2) + (lane >> 4) * 16;
    gload16<0>(xr[0], xb, o); gload16<64>(xr[1], xb, o); }
  unsigned mx;
  gload4<0>(mx, ws + WS_MEANX + tb * 64, (lane & 15) * 4);
  unsigned ra[4];
  const int rrow = (lane >> 4) * 4 + ((lane & 15) >> 2);
  for (int t = 0; t < 4; ++t) ra[t] = sb + rrow * 128 + ((t ^ ((rrow >> 1) & 3)) * 32) + (lane & 3) * 8;
  const unsigned rm = sb + 73728 + (lane & 1) * (D_ * 2) + (lane >> 4) * 16;
  u32x4 s0, s1;
  { const unsigned one = 0x3f80u << (16 * (lane & 1)); const int d = (lane & 7) >> 1, hit = (lane >> 4) - ((lane >> 3) & 1);
    for (int i = 0; i < 4; ++i) { s0[i] = (i == d && hit == 0) ? one : 0u; s1[i] = (i == d && hit == 2) ? one : 0u; }
    opq_v(s0); opq_v(s1); }
  f32x4 acc[5];
  u32x2 A[2][4][2];
  u32x4 Ar[2];
  sfor<0, 19>([&](auto kc) AINL {
    constexpr int k = decltype(kc)::v;
    if constexpr (k % 3 == 0 && k < 18) abar();
    if constexpr (k == 0) { STAMP_MID(1); }
    if constexpr (k == 15) { STAMP_MID(2); }
    if constexpr (k == 16) for (int t = 0; t < 4; ++t) vadd_opq<65536>(ra[t]);
    sfor<0, 5>([&](auto tc) AINL {
      constexpr int t = decltype(tc)::v;
      if constexpr (k < 18 && t < 4) { trd<(k & 15) * 4096>(A[k & 1][t][0], ra[t]); trd<(k & 15) * 4096 + 2048>(A[k & 1][t][1], ra[t]); }
      if constexpr (k < 18 && t == 4) rd128<k * 64>(Ar[k & 1], rm);
      if constexpr (k > 0) {
        wait_vl2<(t == 0 ? 21 - k : 63), (k < 18 ? 9 : t < 4 ? 7 - 2 * t : 0)>();
        if constexpr (t < 4) { const u32x4 av = cat4(A[~k & 1][t][0], A[~k & 1][t][1]); if constexpr (k == 1) mf0(acc[t], av, X[0]); else mf(acc[t], av, X[k - 1]); }
        else { if constexpr (k == 1) mf0(acc[4], Ar[~k & 1], X[0]); else mf(acc[4], Ar[~k & 1], X[k - 1]); }
      }
    });
  });
  STAMP_MID(3);
  asm volatile("s_waitcnt vmcnt(0)" : "+v"(xr[0]), "+v"(xr[1]), "+v"(mx)::"memory");
  for (int t = 0; t < 4; ++t) mf(acc[t], (t & 1) ? s1 : s0, xr[t >> 1]);
  asm volatile("s_nop 7\n\ts_nop 3" : "+v"(acc[0]), "+v"(acc[1]), "+v"(acc[2]), "+v"(acc[3]), "+v"(acc[4]));
  const float mean = __uint_as_float(mx) + (acc[4][0] + acc[4][1]), mb = rbf(mean);
  u32x4 hf[2], hc[2];
  float ss = 0.f;
  for (int t = 0; t < 4; ++t)
    for (int i = 0; i < 2; ++i) {
      const unsigned p = pk2(acc[t][2 * i], acc[t][2 * i + 1]);
      const float a = lo16(p), c = hi16(p);
      hf[t >> 1][(t & 1) * 2 + i] = p;
      hc[t >> 1][(t & 1) * 2 + i] = pk2(a - mb, c - mb);
      ss = __builtin_fmaf(a - mean, a - mean, ss); ss = __builtin_fmaf(c - mean, c - mean, ss);
    }
  { auto r = __builtin_amdgcn_permlane32_swap(__float_as_uint(ss), __float_as_uint(ss), false, false); ss = __uint_as_float(r[0]) + __uint_as_float(r[1]);
    auto q = __builtin_amdgcn_permlane16_swap(__float_as_uint(ss), __float_as_uint(ss), false, false); ss = __uint_as_float(q[0]) + __uint_as_float(q[1]); }
  if (tt * 5 + w < 128) {
    const size_t fo = ((size_t)tb * 18 + cg * 2) * 1024;
    gstore16_wt<0>(ws + WS_HC + fo, lane * 16, hc[0]); gstore16_wt<1024>(ws + WS_HC + fo, lane * 16, hc[1]);
    if (lane < 16) asm volatile("global_store_dword %0, %1, %2 sc1" ::"v"(lane * 4), "v"(ss), "s"(ws + WS_SSQ + cg * 8192 + tb * 64) : "memory");
    gstore16_wt<0>(ws + WS_HF + fo, lane * 16, hf[0]); gstore16_wt<1024>(ws + WS_HF + fo, lane * 16, hf[1]);
  }
  STAMP_END(ws, 1);
}

namespace k4 {
#define K4_N0 6
#define K4_LA 24
#define K4_FAST 2
#define K4_UPL 150
constexpr int N0 = K4_N0, UA = 18 * N0, UB = 126, UE = 162, NR = 41, RR_ = N0 == 7 ? 13 : 14, RING = 8 * RR_, RBO = RING * 1024, RBS = N0 * 1024, LA = K4_LA, UPL = K4_UPL;
constexpr int slot_of(int u) { return u < UA ? u % N0 : u < UB ? 6 : 7 + ((u - UB) & 1); }
constexpr int ks_of(int u) { return u < UA ? u / N0 : u < UB ? u - UA : (u - UB) >> 1; }
}

#define K3_SKIP 0
namespace k3 {
constexpr int NSLOT = 7, C2OFF = NSLOT * 16384;
constexpr int U1 = 144, U2 = 180, U3 = 252, UE = 324;
enum { OW = 0, OX = 1, OC2 = 2, OSQ = 3, OST = 4, OPD = 5, OPS = 6, OFL = 7 };
#define K3_PD 194
#define K3_PR 275
#define K3_PI 8
#define K3_FL 30
#define K3_FI 16
constexpr int upd(int i) { return K3_PD + 2 * i; }
constexpr int upr(int i) { return K3_PR + K3_PI * i; }
constexpr int ups(int i) { return K3_PR + 4 + K3_PI * i; }
constexpr int ufl(int i) { return K3_FL + K3_FI * i; }
enum { VM1 = 0, VEX, VFM, VRC, VM2, VCV, VPL };
struct Sched {
  int n; int Q[160]; int kind[160]; int idx[160];
  int nv; int VQ[400]; int vop[400]; int vsl[400]; int vix[400];
  int stq[7];
  constexpr void add(int q, int k, int i) { Q[n] = q; kind[n] = k; idx[n] = i; ++n; }
  static constexpr int ubar(int j) { return j <= 10 ? 16 * j + 13 : 177 + 16 * (j - 10); }
  static constexpr int uw(int L, int h) {
    if (L <= 8) return 16 * (L - 2) + 2 * h;
    if (L <= 13) return 140 + 8 * (L - 9) + 2 * h;
    return 186 + 16 * (L - 14) + 2 * h;
  }
  static constexpr int uc2(int i) { return i == 0 ? 100 : i == 1 ? 110 : i == 2 ? 118 : i == 3 ? 126 : 134; }
  constexpr int glu(int q, int a, int b, int cap) {
    int used = 0;
    for (int op = VM1; op <= VCV; ++op)
      for (int t = 0; t < (b >= 0 ? 2 : 1); ++t)
        for (int v = 0; v < (op == VCV ? 4 : 8); ++v) {
          int c = (op == VEX || op == VRC) ? 8 : 4;
          if (used && used + c > cap) { ++q; used = 0; }
          VQ[nv] = q; vop[nv] = op; vsl[nv] = t ? b : a; vix[nv] = v; ++nv; used += c;
        }
    return q + 1;
  }
  constexpr Sched() : n(0), Q{}, kind{}, idx{}, nv(0), VQ{}, vop{}, vsl{}, vix{}, stq{} {
    int q = glu(2 * U1 + 2, 0, 1, 8);
    stq[0] = (q + 1) & ~1; stq[1] = stq[0] + 4;
    q = glu(2 * U2 + 2, 2, 3, 4);
    stq[2] = (q + 1) & ~1; stq[3] = stq[2] + 16;
    q = glu(q, 4, -1, 4);
    for (int v = 0; v < 2; ++v) { VQ[nv] = q + 1 + v; vop[nv] = VPL; vsl[nv] = 4; vix[nv] = v; ++nv; }
    stq[4] = (q + 5) & ~1;
    q = glu(2 * U3 + 8, 0, 1, 4);
    stq[5] = (q + 1) & ~1; stq[6] = stq[5] + 16;
    add(-1, OW, 0); add(-1, OW, 1);
    for (int k = 0; k < 4; ++k) add(-1, OX, k);
    for (int i = 0; i < 5; ++i) add(-1, OSQ, i);
    add(-1, OW, 2); add(-1, OW, 3);
    for (int k = 4; k < 8; ++k) add(-1, OX, k);
    for (int qq = 0; qq < 2 * UE; ++qq) {
      int u = qq >> 1, h = qq & 1;
      if (h == 1) for (int L = 2; L < 18; ++L) for (int p = 0; p < 2; ++p) if (uw(L, p) == u) add(qq, OW, 2 * L + p);
      if (h == 1) for (int i = 0; i < 5; ++i) if (uc2(i) == u) add(qq, OC2, i);
      if (h == 0 && u < 16 * 7) { int r = u & 15; if (r == 5 || r == 7 || r == 9 || r == 11) add(qq, OX, 4 * ((u >> 4) + 2) + (r - 5) / 2); }
      for (int t = 0; t < 7; ++t) if (stq[t] == qq) add(qq, OST, t);
      if (h == 1) for (int i = 0; i < 3; ++i) { if (upd(i) == u) add(qq, OPD, i); if (ups(i) == u) add(qq, OPS, i); }
      if (h == 1) for (int i = 0; i < 4; ++i) if (ufl(i) == u) add(qq, OFL, i);
    }
  }
  constexpr int find(int k, int i) const { for (int j = 0; j < n; ++j) if (kind[j] == k && idx[j] == i) return j; return -1000; }
  constexpr int cnt(int q, int k, int i) const { int j = find(k, i), c = 0; for (int m = j + 1; m < n; ++m) if (Q[m] < q) ++c; return c; }
  constexpr int barat(int u) const { for (int j = 0; j < 19; ++j) if (ubar(j) == u) return j; return -1; }
  constexpr int vmat(int u) const {
    int v = 63;
    if (u < U1 && (u & 7) == 0) v = cnt(2 * u, OX, 2 * (u >> 3) + 1);
    int j = barat(u);
    if (j >= 0) { int c = j < 8 ? cnt(2 * u, OW, 2 * (j + 1) + 1) : j == 8 ? cnt(2 * u, OC2, 1) : j == 9 ? cnt(2 * u, OC2, 3) : cnt(2 * u, OW, 2 * (j - 1) + 1); if (c < v) v = c; }
    return v;
  }
  constexpr int opat(int q, int k) const { for (int j = 0; j < n; ++j) if (Q[j] == q && kind[j] == k) return idx[j]; return -1; }
  constexpr int vfirst(int q) const { for (int j = 0; j < nv; ++j) if (VQ[j] >= q) return j; return nv; }
};
constexpr Sched SC{};
static_assert(SC.stq[1] < 2 * U2 && SC.stq[4] < 2 * U3 + 8 && SC.stq[6] < 2 * UE, "schedule");
}

__global__ __launch_bounds__(512, 2) void k3_gateup(const u16* __restrict__ Wg, const u16* __restrict__ Wu, const u16* __restrict__ Wd, char* __restrict__ ws) {
  using namespace k3;
  __shared__ __attribute__((aligned(1024))) char smem[C2OFF + 36864];
  STAMP_BEGIN();
  const unsigned sb = (unsigned)(size_t)smem;
  const int w = __builtin_amdgcn_readfirstlane(threadIdx.x >> 6);
  const int lane = threadIdx.x & 63;
  const int b = blockIdx.x, xcd = b & 7, bi = b >> 3;
  const int tt = (xcd >> 2) * 4 + (bi >> 3);
  const int fg = (xcd & 3) * 8 + (bi & 7);
  const int G = fg >> 2, j = fg & 3;
  const int tb = tt * 16 + 2 * w;
  const int gu = w >> 2;
  const u32x4 rsW = mkrsrc(gu ? Wu : Wg, D_ * F_ * 2);
  unsigned wlo, w2lo;
  { int row = lane >> 3, pos = (lane >> 1) & 3; int tile = pos ^ ((row >> 1) & 3);
    wlo = (row + 16 * (w & 3)) * (F_ * 2) + tile * 32 + (lane & 1) * 16 + (G * 9 + 2 * j) * 128;
    w2lo = ((lane >> 1) + 32 * (w & 3)) * (F_ * 2) + (lane & 1) * 16 + (G * 9 + 8) * 128 + j * 32; }
  unsigned wdst = sb + gu * 8192 + (w & 3) * 2048;
  unsigned wdst2 = sb + gu * 4096 + (w & 3) * 1024;
  unsigned c2dst = sb + C2OFF + gu * 18432 + (w & 3) * 1024;
  u32x4 X[36];
  unsigned sq[5];
  const char* xb[2][3];
  for (int k = 0; k < 2; ++k) for (int i = 0; i < 3; ++i) { xb[k][i] = ws + WS_HC + (size_t)(tb + k) * 18432 + 4096 + i * 8192; opq_s(xb[k][i]); }
  char* ab[2];
  for (int k = 0; k < 2; ++k) { ab[k] = ws + WS_ACT + (size_t)(tb + k) * 144 * 1024 + (size_t)(G * 18 + 4 * j) * 1024; opq_s(ab[k]); }
  char* ab2 = ws + WS_ACT + (size_t)tb * 144 * 1024 + (size_t)(G * 18 + 16 + (j >> 1)) * 1024 + (j & 1) * 512; opq_s(ab2);
  const unsigned l16 = lane * 16;
  char* fb = ws + WS_PART + (size_t)b * PART_BLK + w * 3072; opq_s(fb);
  char* fb2 = ws + WS_PART + (size_t)b * PART_BLK + 24576 + w * 384; opq_s(fb2);
  unsigned so;
  float sdv[2], nav[2];
  f32x4 acc[5][4];
  float gv[5][8];
  unsigned ov[5][4];
  unsigned pso[3], pdo[3], pvo, pk_rd;
  u32x4 rsD, rsP, pkv, ones;
  auto vm_set = [&](auto Qc) AINL {
    constexpr int q = decltype(Qc)::v, iw = SC.opat(q, OW), ic = SC.opat(q, OC2), ip = SC.opat(q, OPD);
    if constexpr (ip >= 0) {
      unsigned ln = l16; opq_v(ln); ln >>= 4;
      const unsigned p = (w & 3) * 8 + (ln >> 3), kn = 8 * ((p >> 2) & 3) + 4 * (p >> 4) + (p & 3);
      pvo = ((pso[ip] & 1) ? kn : p) * (D_ * 2) + ((((ln >> 1) & 3) ^ ((p >> 1) & 3)) * 32) + (ln & 1) * 16; opq_v(pvo);
      pso[ip] &= ~1u; opq_s(pso[ip]);
      dma_set<0, C2OFF + ip * 8192>(so, sb + w * 1024);
    }
    if constexpr (iw >= 0 && iw < 18) dma_set<((iw >> 1) * 64 + (iw & 1) * 8) * (F_ * 2), ((iw >> 1) % NSLOT) * 16384 + (iw & 1) * 1024>(so, wdst);
    if constexpr (iw >= 18) { constexpr int hs = iw - 18;
      dma_set<(hs % 9) * 64 * (F_ * 2) + (hs / 9) * 64, ((iw >> 1) % NSLOT) * 16384 + (iw & 1) * 8192>(so, wdst2); }
    if constexpr (ic >= 0) dma_set<ic * 128 * (F_ * 2), ic * 4096>(so, c2dst);
  };
  auto vm_go = [&](auto Qc) AINL {
    constexpr int q = decltype(Qc)::v, iw = SC.opat(q, OW), ic = SC.opat(q, OC2), ix = SC.opat(q, OX), is = SC.opat(q, OST);
    if constexpr (iw >= 0 && !(K3_SKIP & 1)) dma_go(rsW, wlo, so);
    if constexpr (ic >= 0 && !(K3_SKIP & 2)) dma_go(rsW, w2lo, so);
    constexpr int ipd = SC.opat(q, OPD), ips = SC.opat(q, OPS);
    if constexpr (ipd >= 0 && !(K3_SKIP & 4)) dma_go(rsD, pvo, pso[ipd]);
    if constexpr (ips >= 0 && !(K3_SKIP & 4)) asm volatile("buffer_store_dwordx4 %0, %1, %2, %3 offen sc1\n\ts_nop 1" ::"v"(pkv), "v"(l16), "s"(rsP), "s"(pdo[ips]) : "memory");
    if constexpr (SC.opat(q, OFL) >= 0 && !(K3_SKIP & 16)) {
      constexpr int i = SC.opat(q, OFL);
      if constexpr (i == 0) { ones = u32x4{~0u, ~0u, ~0u, ~0u}; opq_v(ones); }
      if constexpr (i < 3) gstore16_wt<i * 1024>(fb, l16, ones);
      else gstore16_wt<0>(fb2, min(l16, 368u), ones);
    }
    if constexpr (ix >= 0 && !(K3_SKIP & 8)) gload16<((ix >> 1) & 7) * 1024 - 4096>(X[ix], xb[ix & 1][ix >> 4], l16);
    if constexpr (ix >= 0 && (K3_SKIP & 8)) asm volatile("" : "=v"(X[ix]));
    if constexpr (is == 4) {
      const unsigned off = ((lane >> 4) & 1) * (144 * 1024) + (lane >> 5) * 256 + (lane & 15) * 16;
      gstore16_act<0>(ab2, off, u32x4{ov[4][0], ov[4][1], ov[4][2], ov[4][3]});
    } else if constexpr (is >= 0) {
      constexpr int i2 = is > 4 ? is - 1 : is, pr = i2 >> 1, tk = i2 & 1, a = (pr & 1) * 2;
      gstore16_act<pr * 1024>(ab[tk], l16, u32x4{ov[a][2 * tk], ov[a][2 * tk + 1], ov[a + 1][2 * tk], ov[a + 1][2 * tk + 1]});
    }
  };
  auto vop = [&](auto opc, auto slc, auto vc) AINL {
    constexpr int op = decltype(opc)::v, sl = decltype(slc)::v, v = decltype(vc)::v, tk = v >> 2;
    if constexpr (op == VM1) a_mul(gv[sl][v], acc[sl][tk][v & 3], nav[tk]);
    else if constexpr (op == VEX) a_exp(gv[sl][v]);
    else if constexpr (op == VFM) a_fma11(gv[sl][v], sdv[tk]);
    else if constexpr (op == VRC) a_rcp(gv[sl][v]);
    else if constexpr (op == VM2) a_mul(gv[sl][v], gv[sl][v], acc[sl][2 + tk][v & 3]);
    else if constexpr (op == VCV) a_cvt(ov[sl][v], gv[sl][2 * v], gv[sl][2 * v + 1]);
    else a_pl16(ov[sl][v], ov[sl][2 + v]);
  };
  auto valu = [&](auto Qc) AINL {
    constexpr int q = decltype(Qc)::v;
    sfor<SC.vfirst(q), SC.vfirst(q + 1)>([&](auto jc) AINL { constexpr int jj = decltype(jc)::v; vop(IC<SC.vop[jj]>{}, IC<SC.vsl[jj]>{}, IC<SC.vix[jj]>{}); });
  };
  sfor<0, 17>([&](auto jc) AINL {
    constexpr int jj = decltype(jc)::v, kd = SC.kind[jj], id = SC.idx[jj];
    static_assert(SC.Q[jj] == -1 && SC.Q[17] >= 0, "prologue");
    if constexpr (kd == OW) { dma_set<((id >> 1) * 64 + (id & 1) * 8) * (F_ * 2), (id >> 1) * 16384 + (id & 1) * 1024>(so, wdst); dma_go(rsW, wlo, so); }
    else if constexpr (kd == OX) gload16<((id >> 1) & 7) * 1024 - 4096>(X[id], xb[id & 1][id >> 4], l16);
    else if constexpr (kd == OSQ) gload4<0>(sq[id], ws + WS_SSQ + (size_t)tb * 64 + id * 2 * 8192, (lane & 31) * 4 + (lane >> 5) * 8192);
  });

  rsD = mkrsrc(Wd, b < 216 ? D_ * F_ * 2 : 0); rsP = mkrsrc(ws + WS_WDP, b < 216 ? D_ * F_ * 2 : 0);
  { const unsigned n = 6 * b + 2 * (lane & 3) + (w >> 2), ksg = n / 9, T = (n - ksg * 9) * 4 + (w & 3);
    const unsigned q = ksg / 36, kk = ksg - q * 36, kh = kk >= 18, ks = kk - 18 * kh, cg = T / 9, lt = T - cg * 9 - (cg & 1);
    const unsigned sl = (((lt >> 1) - q - 1) & 3) * 2 + (lt & 1);
    const unsigned u = lt >= 8 ? (k4::N0 == 7 ? ks * 7 + 6 : k4::UA + ks) : sl < 6 ? ks * k4::N0 + sl : k4::UB + 2 * ks + sl - 6;
    const unsigned a = ksg * 32 * (D_ * 2) + (n - ksg * 9) * 128 + (ks >= 16), d = ((q * 4 + cg) * 324 + 2 * u + kh) * 1024;
    for (int i = 0; i < 3; ++i) { pso[i] = __builtin_amdgcn_readlane(a, i); pdo[i] = __builtin_amdgcn_readlane(d, i); opq_s(pso[i]); opq_s(pdo[i]); } }
  opq_s(rsD); opq_s(rsP);
  unsigned ra[4];
  const int rrow = (lane >> 4) * 4 + ((lane & 15) >> 2);
  for (int t = 0; t < 4; ++t) ra[t] = sb + rrow * 128 + ((t ^ ((rrow >> 1) & 3)) * 32) + (lane & 3) * 8;
  u32x2 A[4][2];
  auto ldA = [&](auto uc, auto hc) AINL {
    constexpr int u = decltype(uc)::v, h = decltype(hc)::v;
    if constexpr (u < U1) {
      constexpr int t = u & 3, g = u >> 4, k = (u >> 2) & 3, slot = g % NSLOT;
      trd<(slot & 3) * 16384 + (k & 1) * 8192 + (k >> 1) * 4096 + h * 2048>(A[u & 3][h], ra[t]);
    } else if constexpr (u < U2) {
      constexpr int n = u - U1;
      trd<(n & 1) * 18432 + (n >> 1) * 1024 + h * 512>(A[u & 3][h], ra[0]);
    } else if constexpr (u < UE) {
      constexpr int n = u - U2, slot = (9 + (n >> 4)) % NSLOT;
      trd<(slot & 3) * 16384 + ((n >> 3) & 1) * 8192 + ((n >> 1) & 1) * 4096 + ((n >> 2) & 1) * 2048 + h * 1024>(A[u & 3][h], ra[2 + (n & 1)]);
    }
  };
  auto fixA = [&](auto uc) AINL {
    constexpr int u = decltype(uc)::v;
    if constexpr (u < U1 && u >= 16 && (u & 15) < 4) {
      constexpr int g = u >> 4, a = (g % NSLOT) >> 2, p = ((g - 1) % NSLOT) >> 2;
      if constexpr (a != p) vadd_opq<(a - p) * 65536>(ra[u & 3]);
    }
    if constexpr (u == U1) { ra[0] = sb + C2OFF + rrow * 32 + (lane & 3) * 8; opq_v(ra[0]); }
    if constexpr (u == U2 || u == U2 + 1) { ra[2 + u - U2] = sb + rrow * 64 + (((u - U2) ^ ((rrow >> 2) & 1)) * 32) + (lane & 3) * 8; opq_v(ra[2 + u - U2]); }
    if constexpr (u >= U2 + 16 && u < UE && ((u - U2) & 15) < 2) {
      constexpr int g = 9 + ((u - U2) >> 4), a = (g % NSLOT) >> 2, p = ((g - 1) % NSLOT) >> 2;
      if constexpr (a != p) vadd_opq<(a - p) * 65536>(ra[2 + (u & 1)]);
    }
  };
  wait_vm<SC.cnt(0, OW, 1)>();
  abar();
  STAMP_MID(1);
  sfor<0, 3>([&](auto uc) AINL { ldA(uc, IC<0>{}); ldA(uc, IC<1>{}); });
  sfor<0, UE>([&](auto uc) AINL {
    constexpr int u = decltype(uc)::v;
    if constexpr (u == U1) { STAMP_MID(2); }
    if constexpr (u == U2) { STAMP_MID(3); }
    if constexpr (u == U3) { STAMP_MID(4); }
    if constexpr (u == Sched::uc2(4)) {
      if ((w & 3) >= 2) { w2lo -= 64 * (F_ * 2); c2dst -= 2048; }
      opq_v(w2lo); opq_s(c2dst);
    }
    if constexpr (u == Sched::uw(9, 0)) {
      int row = lane >> 2, tile = ((lane >> 1) & 1) ^ ((row >> 2) & 1);
      wlo = (row + 16 * (w & 3)) * (F_ * 2) + tile * 32 + (lane & 1) * 16 + (G * 9 + 2 * j + 1) * 128; opq_v(wlo);
    }
    wait_vl<SC.vmat(u), (u >= UE - 1 ? 0 : u == UE - 2 ? 2 : 4)>();
    if constexpr (SC.barat(u) >= 0) abar();
    if constexpr (u == 16) {
      opq_v(sq[0]); opq_v(sq[1]); opq_v(sq[2]); opq_v(sq[3]); opq_v(sq[4]);
      float s = (__uint_as_float(sq[0]) + __uint_as_float(sq[1])) + (__uint_as_float(sq[2]) + __uint_as_float(sq[3])) + (lane < 32 ? __uint_as_float(sq[4]) : 0.f);
      auto sw = __builtin_amdgcn_permlane32_swap(__float_as_uint(s), __float_as_uint(s), false, false);
      s = __uint_as_float(sw[0]) + __uint_as_float(sw[1]);
      float var = rbf(s * (1.f / D_));
      float sd = rbf(__builtin_sqrtf(rbf(var + 1e-5f)));
      auto s2 = __builtin_amdgcn_permlane16_swap(__float_as_uint(sd), __float_as_uint(sd), false, false);
      sdv[0] = __uint_as_float(s2[0]); sdv[1] = __uint_as_float(s2[1]);
      nav[0] = -1.44269504088896f * __builtin_amdgcn_rcpf(sdv[0]); nav[1] = -1.44269504088896f * __builtin_amdgcn_rcpf(sdv[1]);
      opq_v(sdv[0]); opq_v(sdv[1]); opq_v(nav[0]); opq_v(nav[1]);
    }
    constexpr int sl = u < U1 ? (u & 3) : u < U2 ? 4 : u < U3 ? (u & 1) : 2 + (u & 1);
    constexpr int m2 = u < U1 ? ((u >> 2) & 1) * 2 : u < U2 ? (u & 1) * 2 : (u & 2);
    constexpr int ks = u < U1 ? u >> 3 : u < U2 ? (u - U1) >> 1 : u < U3 ? (u - U2) >> 2 : (u - U3) >> 2;
    const u32x4 av = cat4(A[u & 3][0], A[u & 3][1]);
    if constexpr (ks == 0) mf0(acc[sl][m2], av, X[0]); else mf(acc[sl][m2], av, X[2 * ks]);
    ldA(IC<u + 3>{}, IC<0>{});
    if constexpr (u == upr(0) - 1) { unsigned ln = l16; opq_v(ln); ln >>= 4; const unsigned rr = (ln >> 4) * 4 + ((ln & 15) >> 2);
      pk_rd = sb + C2OFF + (w >> 2) * 4096 + rr * 128 + (((w & 3) ^ ((rr >> 1) & 3)) * 32) + (ln & 3) * 8; opq_v(pk_rd); }
    if constexpr (u == upr(0) || u == upr(1) || u == upr(2)) { constexpr int i = (u - upr(0)) / K3_PI;
      u32x2 a, c; trd<i * 8192>(a, pk_rd); trd<i * 8192 + 2048>(c, pk_rd); pkv = cat4(a, c); }
    valu(IC<2 * u>{});
    vm_set(IC<2 * u + 1>{});
    vm_go(IC<2 * u>{});
    if constexpr (ks == 0) mf0(acc[sl][m2 + 1], av, X[1]); else mf(acc[sl][m2 + 1], av, X[2 * ks + 1]);
    ldA(IC<u + 3>{}, IC<1>{});
    valu(IC<2 * u + 1>{});
    vm_go(IC<2 * u + 1>{});
    fixA(IC<u + 4>{});
  });
  STAMP_MID(5);
  asm volatile("s_nop 7\n\ts_nop 7");
  sfor<VM1, VCV + 1>([&](auto opc) AINL {
    sfor<2, 4>([&](auto slc) AINL { sfor<0, (decltype(opc)::v == VCV ? 4 : 8)>([&](auto vc) AINL { vop(opc, slc, vc); }); });
  });
  sfor<0, 2>([&](auto tc) AINL { constexpr int tk = decltype(tc)::v;
    gstore16_act<3 * 1024>(ab[tk], l16, u32x4{ov[2][2 * tk], ov[2][2 * tk + 1], ov[3][2 * tk], ov[3][2 * tk + 1]}); });
  STAMP_MID(6);
  STAMP_END(ws, 2);
}

namespace k4 {
enum { OA = 0, ORW, ORR, OADD, OCVT, OWS, OW, OX, OH, OPS, OPL, NK };
constexpr int QO = 2 * LA + 8, NQ = 2 * UE + QO + 1;
struct Sched {
  int n; short Q[640], K[640], I[640];
  short first[NQ + 1];
  short loc[NK][170];
  short cvm[641], clg[641];
  signed char vm[2 * UE + 1], lg[2 * UE + 1], bar[UE + 1];
  int n0; short q0[640], k0[640], i0[640];
  int vm0;
  constexpr void add(int q, int k, int i) { q0[n0] = q; k0[n0] = k; i0[n0] = i; ++n0; }
  constexpr int cnt(int q, int k, int i) const {
    int j = loc[k][i], f = first[q + QO];
    return k >= OW ? cvm[f] - cvm[j + 1] : clg[f] - clg[j + 1];
  }
  constexpr void need(int q, int k, int i) {
    int c = cnt(q, k, i);
    if (Q[loc[k][i]] >= q) c = -100;
    if (k >= OW) { if (c < vm[q]) vm[q] = c; } else { if (c < lg[q]) lg[q] = c; }
  }
  constexpr Sched() : n(0), Q{}, K{}, I{}, first{}, loc{}, cvm{}, clg{}, vm{}, lg{}, bar{}, n0(0), q0{}, k0{}, i0{}, vm0(0) {
    int ub[40] = {}, nb = 0;
    int sbar[2] = {UA + 3, UB + 3};
    for (int u = 5; u < UA - 4; u += 8) ub[nb++] = u;
    ub[nb] = UA - 3 > ub[nb - 1] + 2 ? UA - 3 : 0; if (ub[nb]) ++nb;
    ub[nb++] = UA + 3;
    if (UA < UB) { ub[nb++] = UA + 11; ub[nb++] = UB + 3; }
    ub[nb++] = UB + 11; ub[nb++] = UB + 19;
    for (int j = 0; j < nb; ++j) bar[ub[j]] = 1;
    for (int u = 3; u < UE; ++u) add(2 * (u - 3), OA, u);
    int qadd[7] = {}, qps[4] = {};
    for (int g = 0; g < 2; ++g) {
      int a = g ? 6 : 0, b = g ? 7 : N0, E = g ? UB : UA;
      if (g && N0 == 7) break;
      for (int s = a; s < b; ++s) add(2 * (E - (b - s)) + 4, ORW, s);
      int q = 2 * sbar[g];
      for (int s = a; s < b; ++s) add(q + s - a, ORR, s);
      q += 4;
      for (int s = a; s < b; ++s) {
        qadd[s] = q;
        for (int i = 0; i < 4; ++i) add(q + (i >> 1), OADD, 4 * s + i);
        for (int i = 0; i < 2; ++i) add(q + 2, OCVT, 2 * s + i);
        q += 3;
        if ((s & 1) || s == 6) qps[s >> 1] = q;
      }
    }
    int pw = -1000;
    for (int r = 0; r < NR; ++r) {
      int u = 4 * r - LA;
      if (4 * r >= UA) u = UA - LA + (4 * r - UA) / K4_FAST;
      if (r >= RR_) { int j = 0; while (ub[j] < 4 * (r - RR_) + 3) ++j; if (u < ub[j]) u = ub[j]; }
      if (2 * u + 1 < pw + 2) u = (pw + 1) / 2;
      pw = 2 * u + 1;
      add(pw - 1, OWS, r); add(pw, OW, r);
    }
    for (int i = 0; i < 36; ++i) add(2 * (N0 * (i >> 1) - LA + 2 * (i & 1)) + 2, OX, i);
    for (int i = 0; i < 2; ++i) add(2 * (UPL - 8) + 2 * i, OH, i);
    for (int i = 0; i < 4; ++i) add(qps[i], OPS, i);
    for (int i = 0; i < 6; ++i) add(2 * UPL + 2 * i, OPL, i);
    short cn[NQ + 1] = {};
    for (int j = 0; j < n0; ++j) ++cn[q0[j] + QO];
    int acc = 0;
    for (int q = 0; q <= NQ; ++q) { first[q] = acc; acc += cn[q]; cn[q] = first[q]; }
    for (int j = 0; j < n0; ++j) { int p = cn[q0[j] + QO]++; Q[p] = q0[j]; K[p] = k0[j]; I[p] = i0[j]; loc[k0[j]][i0[j]] = p; }
    n = n0;
    for (int j = 0; j < n; ++j) { cvm[j + 1] = cvm[j] + (K[j] >= OW); clg[j + 1] = clg[j] + (K[j] <= ORR); }
    for (int q = 0; q <= 2 * UE; ++q) { vm[q] = 63; lg[q] = 15; }
    for (int u = 3; u < UE; ++u) need(2 * u, OA, u);
    for (int k = 1; k < 18; ++k) need(2 * N0 * k, OX, 2 * k + 1);
    for (int j = 0; j < nb; ++j) { int r = j + 1 < nb ? (ub[j + 1] + 2) / 4 : NR - 1; need(2 * ub[j], OW, r); }
    need(2 * sbar[0], ORW, N0 - 1);
    if (N0 < 7) need(2 * sbar[1], ORW, 6);
    for (int s = 0; s < 7; ++s) need(qadd[s], ORR, s);
    int c1 = cnt(0, OW, (ub[0] + 2) / 4), c2 = cnt(0, OX, 1);
    vm0 = c1 < c2 ? c1 : c2;
  }
  constexpr bool ok() const { for (int q = 0; q <= 2 * UE; ++q) if (vm[q] < 0 || lg[q] < 0) return false; return Q[n - 1] < 2 * UE; }
};
constexpr Sched SC{};
static_assert(SC.ok(), "schedule");
}

__global__ __launch_bounds__(512, 2) void k4_down(char* __restrict__ ws, u16* __restrict__ out) {
  using namespace k4;
  __shared__ __attribute__((aligned(1024))) char smem[RBO + 8 * RBS];
  STAMP_BEGIN();
  const unsigned sb = (unsigned)(size_t)smem;
  const int w = __builtin_amdgcn_readfirstlane(threadIdx.x >> 6);
  const int lane = threadIdx.x & 63;
  const int b = blockIdx.x, q = b & 3, bi = b >> 3;
  const int tt = ((b >> 2) & 1) * 8 + (bi >> 2), cg = bi & 3;
  const int m = w >> 1, kh = w & 1;
  const int tb = tt * 8 + w;
  const u32x4 rsW = mkrsrc(ws + WS_WDP + (size_t)(q * 4 + cg) * (324 * 1024), 324 * 1024);
  const unsigned l16 = lane * 16;
  const unsigned wlo = l16 + w * 1024;
  const unsigned wdst = sb + w * 1024;
  const char* xb[2][3];
  for (int k = 0; k < 2; ++k) for (int i = 0; i < 3; ++i) { xb[k][i] = ws + WS_ACT + ((size_t)(tb ^ k) * 144 + q * 36 + kh * 18) * 1024 + 4096 + i * 8192; opq_s(xb[k][i]); }
  unsigned ra[2] = {sb + kh * 1024 + l16, sb + kh * 1024 + l16 + 65536};
  unsigned rwa = sb + RBO + w * RBS + l16, rra = sb + RBO + (w ^ 1) * RBS + l16;
  opq_v(ra[0]); opq_v(ra[1]); opq_v(rwa); opq_v(rra);
  char* pb = ws + WS_PART + (size_t)(b & ~3) * PART_BLK;
  char* psb[4]; const char* plb[2]; const char* hb[2];
  for (int i = 0; i < 3; ++i) { psb[i] = pb + (size_t)((q + 1 + i) & 3) * PART_BLK + w * 3072 + i * 1024; opq_s(psb[i]); }
  psb[3] = pb + (size_t)m * PART_BLK + 24576 + ((m - q - 1) & 3) * 1024 + kh * 512; opq_s(psb[3]);
  plb[0] = pb + (size_t)q * PART_BLK + w * 3072; plb[1] = pb + (size_t)q * PART_BLK + 24576 + kh * 512; opq_s(plb[0]); opq_s(plb[1]);
  hb[0] = ws + WS_HF + ((size_t)tb * 18 + (cg >> 1) * 9 + (cg & 1) * 5 + q) * 1024;
  hb[1] = ws + WS_HF + ((size_t)tb * 18 + (cg >> 1) * 9 + 4) * 1024 + (cg & 1) * 8; opq_s(hb[0]); opq_s(hb[1]);
  char* ob = (char*)out + (size_t)tb * 16 * 1152 + (cg >> 1) * 576;
  char* ob0 = ob + ((cg & 1) * 5 + q) * 64; char* ob1 = ob + 256 + (cg & 1) * 32; opq_s(ob0); opq_s(ob1);
  unsigned l8 = lane * 8, l8m = m == q ? lane * 8 : 0; opq_v(l8); opq_v(l8m);
  u32x4 X[36], A[4], P[4], sel0, sel1, selb;
  u32x2 Ps[4];
  f32x4 acc[9][2], rr[7];
  unsigned ov[7][2], oo0, oo1;
  unsigned so;
  auto op = [&](auto jc) AINL {
    constexpr int j = decltype(jc)::v, k = SC.K[j], i = SC.I[j];
    if constexpr (k == OA) { constexpr int o = (2 * i % RING) * 1024; rd128<(o & 65535)>(A[i & 3], ra[o >> 16]); }
    else if constexpr (k == ORW) wr128<i % N0 * 1024>(rwa, acc[i][1]);
    else if constexpr (k == ORR) rd128f<i % N0 * 1024>(rr[i], rra);
    else if constexpr (k == OADD) { float t = acc[i >> 2][0][i & 3]; a_add(t, rr[i >> 2][i & 3]); acc[i >> 2][0][i & 3] = t; }
    else if constexpr (k == OCVT) a_cvt(ov[i >> 1][i & 1], acc[i >> 1][0][2 * (i & 1)], acc[i >> 1][0][2 * (i & 1) + 1]);
    else if constexpr (k == OWS) dma_set<i * 8192, (8 * i % RING) * 1024>(so, wdst);
    else if constexpr (k == OW) dma_go(rsW, wlo, so);
    else if constexpr (k == OX) gload16<((i >> 1) & 7) * 1024 - 4096>(X[i], xb[i & 1][i >> 4], l16);
    else if constexpr (k == OH) { if constexpr (i == 0) gload16<0>(P[3], hb[0], l16); else gload8<0>(Ps[3], hb[1], l16); }
    else if constexpr (k == OPS) {
      if constexpr (i < 3) gstore16_wt<0>(psb[i], l16, u32x4{ov[2 * i][0], ov[2 * i][1], ov[2 * i + 1][0], ov[2 * i + 1][1]});
      else gstore8_wt<0>(psb[3], l8, u32x2{ov[6][0], ov[6][1]});
    } else if constexpr (k == OPL) {
      if constexpr (i < 3) gload16_c<i * 1024>(P[i], plb[0], l16); else gload8_c<(i - 3) * 1024>(Ps[i - 3], plb[1], l8m);
    }
  };
  sfor<0, SC.first[QO]>(op);
  wait_vm<SC.vm0>();
  abar();
  STAMP_MID(1);
  sfor<0, 3>([&](auto uc) AINL { constexpr int u = decltype(uc)::v; rd128<2048 * u>(A[u], ra[0]); });
  sfor<0, 2 * UE>([&](auto qc) AINL {
    constexpr int g = decltype(qc)::v, u = g >> 1, i = g & 1, sl = slot_of(u), ks = ks_of(u);
    if constexpr (g == 2 * UA) { STAMP_MID(2); }
    if constexpr (g == 2 * UB + 16) { STAMP_MID(3); }
    wait_vl2<SC.vm[g], (g < 6 && i == 0 ? 2 : SC.lg[g])>();
    if constexpr (i == 0 && SC.bar[u]) abar();
    if constexpr (ks == 0) mf0(acc[sl][i], A[u & 3], X[i]); else mf(acc[sl][i], A[u & 3], X[2 * ks + i]);
    sfor<SC.first[g + QO], SC.first[g + 1 + QO]>(op);
    if constexpr (g == 2 * UE - 21) {
      opq_v(l8);
      const unsigned one = ((l8 >> 5) & 3) == (l8 >> 7) ? 0x3f80u << ((l8 & 8) * 2) : 0u;
      const unsigned d0 = (l8 & 16) ? 0u : one, d1 = (l8 & 16) ? one : 0u;
      selb = u32x4{d0, d1, d0, d1}; opq_v(selb);
    }
    if constexpr (g == 2 * UE - 17) { sel0 = u32x4{selb[0], selb[1], 0u, 0u}; opq_v(sel0); }
    if constexpr (g == 2 * UE - 15) { sel1 = u32x4{0u, 0u, selb[0], selb[1]}; opq_v(sel1); }
    if constexpr (g == 2 * UE - 13) { opq_v(l8); oo1 = ((l8 >> 3) & 15) * 1152; oo0 = oo1 + ((l8 >> 7) & 1) * 32 + (l8 >> 8) * 16; oo1 += (l8 >> 7) * 8; opq_v(oo0); opq_v(oo1); }
  });
  STAMP_MID(4);
  asm volatile("s_nop 7\n\ts_nop 3");
  wr128<1024>(rwa, acc[7][1]); wr128<2048>(rwa, acc[8][1]);
  wait_vl2<63, 0>();
  abar();
  rd128f<1024>(rr[0], rra); rd128f<2048>(rr[1], rra);
  asm volatile("s_waitcnt vmcnt(0) lgkmcnt(0)" : "+v"(P[0]), "+v"(P[1]), "+v"(P[2]), "+v"(P[3]), "+v"(Ps[0]), "+v"(Ps[1]), "+v"(Ps[2]), "+v"(Ps[3]), "+v"(rr[0]), "+v"(rr[1])::"memory");
  auto missing = [&]() AINL {
    unsigned mx = umax3(umax3(P[0][0], P[0][1], P[0][2]), umax3(P[0][3], P[1][0], P[1][1]), umax3(P[1][2], P[1][3], P[2][0]));
    mx = umax3(mx, P[2][1], __builtin_elementwise_max(P[2][2], P[2][3]));
    if (m == q) mx = umax3(mx, umax3(Ps[0][0], Ps[0][1], Ps[1][0]), umax3(Ps[1][1], Ps[2][0], Ps[2][1]));
    return __builtin_amdgcn_ballot_w64(mx == 0xffffffffu) != 0;
  };
#pragma unroll 1
  for (int spin = 0; __builtin_expect(missing(), 0) && spin < (1 << 16); ++spin) {
    asm volatile("s_sleep 1\n\tglobal_load_dwordx4 %0, %6, %8 sc1\n\tglobal_load_dwordx4 %1, %6, %8 offset:1024 sc1\n\tglobal_load_dwordx4 %2, %6, %8 offset:2048 sc1\n\t"
                 "global_load_dwordx2 %3, %7, %9 sc1\n\tglobal_load_dwordx2 %4, %7, %9 offset:1024 sc1\n\tglobal_load_dwordx2 %5, %7, %9 offset:2048 sc1\n\ts_waitcnt vmcnt(0)"
                 : "+v"(P[0]), "+v"(P[1]), "+v"(P[2]), "+v"(Ps[0]), "+v"(Ps[1]), "+v"(Ps[2]) : "v"(l16), "v"(l8m), "s"(plb[0]), "s"(plb[1]) : "memory");
  }
  asm volatile("s_nop 1" : "+v"(P[0]), "+v"(P[1]), "+v"(P[2]), "+v"(Ps[0]), "+v"(Ps[1]), "+v"(Ps[2]), "+v"(Ps[3]));
  STAMP_MID(5);
  sfor<0, 4>([&](auto ic) AINL { constexpr int i = decltype(ic)::v; mf(acc[7][0], sel0, P[i]); mf(acc[8][0], sel1, P[i]); });
  if (m == q) { mf(acc[6][0], selb, cat4(Ps[0], Ps[1])); mf(acc[6][0], selb, cat4(Ps[2], Ps[3])); }
  asm volatile("s_nop 7\n\ts_nop 3" : "+v"(acc[6][0]), "+v"(acc[7][0]), "+v"(acc[8][0])::"memory");
  { const f32x4 a = acc[7][0] + rr[0], c = acc[8][0] + rr[1];
    auto s0 = __builtin_amdgcn_permlane16_swap(pk2(a[0], a[1]), pk2(c[0], c[1]), false, false);
    auto s1 = __builtin_amdgcn_permlane16_swap(pk2(a[2], a[3]), pk2(c[2], c[3]), false, false);
    gstore16_wt<0>(ob0, oo0, u32x4{s0[0], s1[0], s0[1], s1[1]});
    if (m == q) gstore8_wt<0>(ob1, oo1, u32x2{pk2(acc[6][0][0], acc[6][0][1]), pk2(acc[6][0][2], acc[6][0][3])});
  }
  STAMP_END(ws, 3);
  asm volatile("k_text1:");
}

static constexpr int g_mask = 15;
extern "C" void kernel_launch(void* const* d_in, const int* in_sizes, int n_in, void* d_out, int out_size, void* d_ws, size_t ws_size,
                              hipStream_t stream) {
  const u16* x = (const u16*)d_in[0];
  const u16* wqkv = (const u16*)d_in[1];
  const u16* wo = (const u16*)d_in[2];
  const u16* wg = (const u16*)d_in[4];
  const u16* wu = (const u16*)d_in[5];
  const u16* wd = (const u16*)d_in[6];
  char* ws = (char*)d_ws;
  if (g_mask & 1) k1_attn<<<256, 512, 0, stream>>>(x, wqkv, wo, wg, wu, wd, ws);
  if (g_mask & 2) k2_proj<<<256, 512, 0, stream>>>(x, wo, wg, wu, wd, ws);
  if (g_mask & 4) k3_gateup<<<256, 512, 0, stream>>>(wg, wu, wd, ws);
  if (g_mask & 8) k4_down<<<256, 512, 0, stream>>>(ws, (u16*)d_out);
}
```

```cpp
#include <hip/hip_runtime.h>
#include <stdint.h>

typedef unsigned short u16;
typedef unsigned long long u64;
typedef __attribute__((ext_vector_type(8))) __bf16 bf16x8;
typedef __attribute__((ext_vector_type(2))) __bf16 bf16x2;
typedef __attribute__((ext_vector_type(4))) short s16x4;
typedef __attribute__((ext_vector_type(4))) float f32x4;
typedef __attribute__((ext_vector_type(2))) float f32x2;
typedef __attribute__((ext_vector_type(16))) float f32x16;
typedef __attribute__((ext_vector_type(4))) unsigned u32x4;
typedef __attribute__((ext_vector_type(2))) unsigned u32x2;
#define DI __device__ __forceinline__
#define LDSP(T, a) ((__attribute__((address_space(3))) T*)(a))
#define AINL __attribute__((always_inline))

template <int I> struct IC { static constexpr int v = I; };
template <int B, int E, class F> DI void sfor(F&& f) {
  if constexpr (B + 1 == E) f(IC<B>{});
  else if constexpr (B < E) { sfor<B, (B + E) / 2>(f); sfor<(B + E) / 2, E>(f); }
}

constexpr int S_ = 2048, D_ = 576, F_ = 4608;

constexpr size_t WS_OF   = 0;
constexpr size_t WS_HC   = WS_OF + (size_t)S_ * D_ * 2;
constexpr size_t WS_HF   = WS_HC + (size_t)S_ * D_ * 2;
constexpr size_t WS_ACT  = WS_HF + (size_t)S_ * D_ * 2;
constexpr size_t WS_PART = WS_ACT + (size_t)S_ * F_ * 2;
constexpr size_t PART_BLK = 28672;
constexpr size_t WS_WDP  = WS_PART + (size_t)256 * PART_BLK;
constexpr size_t WS_SSQ  = WS_WDP + (size_t)16 * 324 * 1024;
constexpr size_t WS_OL   = WS_SSQ + (size_t)10 * S_ * 4;
constexpr int OLB = 2176;
constexpr size_t WS_RM   = WS_OL + 128 * OLB;
constexpr size_t WS_FL   = WS_RM + 4096;
constexpr size_t WS_FR   = WS_FL + 152 * 512;
constexpr size_t WS_STAMP = WS_FR + 8 * 2048;

#define STAMP_BEGIN()
#define STAMP_MID(i)
#define STAMP_END(ws, k)
#define STAMP_ARG
#define STAMP_PASS

DI float bf2f(u16 v) { return __uint_as_float(((unsigned)v) << 16); }
DI float rbf(float f) { return (float)(__bf16)f; }
DI unsigned pk2(float a, float b) { bf16x2 v = {(__bf16)a, (__bf16)b}; return __builtin_bit_cast(unsigned, v); }
DI float lo16(unsigned u) { return __uint_as_float(u << 16); }
DI float hi16(unsigned u) { return __uint_as_float(u & 0xffff0000u); }

template <int N> DI void wait_vm() { asm volatile("s_waitcnt vmcnt(%0)" ::"i"(N) : "memory"); }
template <int N> DI void wait_vm_tie(u32x4& a) { asm volatile("s_waitcnt vmcnt(%1)" : "+v"(a) : "i"(N) : "memory"); }
DI void barrier() { __builtin_amdgcn_s_barrier(); }
#define SCHED() __builtin_amdgcn_sched_barrier(0)

DI void glds16(const void* sbase, unsigned voff, unsigned lds) {
  asm volatile("s_mov_b32 m0, %2\n\ts_nop 0\n\tglobal_load_lds_dwordx4 %0, %1" ::"v"(voff), "s"(sbase), "s"(lds) : "memory");
}
template <int C> DI void vadd_opq(unsigned& a) { asm volatile("v_add_u32 %0, %1, %0" : "+v"(a) : "i"(C)); }
template <int OFF> DI void gload16(u32x4& d, const void* sbase, unsigned voff) {
  asm volatile("global_load_dwordx4 %0, %1, %2 offset:%3" : "=v"(d) : "v"(voff), "s"(sbase), "i"(OFF) : "memory");
}
template <int OFF> DI void gload4(unsigned& d, const void* sbase, unsigned voff) {
  asm volatile("global_load_dword %0, %1, %2 offset:%3" : "=v"(d) : "v"(voff), "s"(sbase), "i"(OFF) : "memory");
}
template <int OFF> DI void gstore16_wt(void* sbase, unsigned voff, u32x4 v) {
  asm volatile("global_store_dwordx4 %0, %1, %2 offset:%3 sc1\n\ts_nop 1" ::"v"(voff), "v"(v), "s"(sbase), "i"(OFF) : "memory");
}
#define ACT_POL "sc1"
template <int OFF> DI void gstore16_act(void* sbase, unsigned voff, u32x4 v) {
  asm volatile("global_store_dwordx4 %0, %1, %2 offset:%3 " ACT_POL "\n\ts_nop 1" ::"v"(voff), "v"(v), "s"(sbase), "i"(OFF) : "memory");
}
DI s16x4 trread(unsigned a) { return __builtin_amdgcn_ds_read_tr16_b64_v4i16(LDSP(s16x4, a)); }
DI bf16x8 cat8(s16x4 a, s16x4 b) { return __builtin_bit_cast(bf16x8, __builtin_shufflevector(a, b, 0, 1, 2, 3, 4, 5, 6, 7)); }
DI f32x16 mfma32(bf16x8 a, u32x4 b, f32x16 c) { return __builtin_amdgcn_mfma_f32_32x32x16_bf16(a, __builtin_bit_cast(bf16x8, b), c, 0, 0, 0); }
DI f32x4 mfma16(bf16x8 a, u32x4 b, f32x4 c) { return __builtin_amdgcn_mfma_f32_16x16x32_bf16(a, __builtin_bit_cast(bf16x8, b), c, 0, 0, 0); }

DI void mf(f32x4& c, const u32x4& a, const u32x4& b) { asm volatile("v_mfma_f32_16x16x32_bf16 %0, %1, %2, %0" : "+v"(c) : "v"(a), "v"(b)); }
DI void mf0(f32x4& c, const u32x4& a, const u32x4& b) { asm volatile("v_mfma_f32_16x16x32_bf16 %0, %1, %2, 0" : "=v"(c) : "v"(a), "v"(b)); }
template <int OFF> DI void trd(u32x2& d, unsigned a) { asm volatile("ds_read_b64_tr_b16 %0, %1 offset:%2" : "=v"(d) : "v"(a), "i"(OFF)); }
template <int OFF> DI void rd128(u32x4& d, unsigned a) { asm volatile("ds_read_b128 %0, %1 offset:%2" : "=v"(d) : "v"(a), "i"(OFF)); }
template <int V, int L> DI void wait_vl() {
  if constexpr (V >= 63) asm volatile("s_waitcnt lgkmcnt(%0)" ::"i"(L) : "memory");
  else asm volatile("s_waitcnt vmcnt(%0) lgkmcnt(%1)" ::"i"(V), "i"(L) : "memory");
}
DI void abar() { asm volatile("s_barrier" ::: "memory"); }
DI u32x4 mkrsrc(const void* p, unsigned bytes) { u32x4 r = {(unsigned)(size_t)p, (unsigned)((size_t)p >> 32) & 0xffffu, bytes, 0x20000u}; return r; }
template <int SO, int LO> DI void dma_set(unsigned& so, unsigned ldsbase) {
  asm volatile("s_add_u32 m0, %1, %3\n\ts_mov_b32 %0, %2" : "=s"(so) : "s"(ldsbase), "i"(SO), "i"(LO) : "scc");
}
DI void dma_setr(unsigned lds) { asm volatile("s_mov_b32 m0, %0" ::"s"(lds)); }
DI void dma_go(const u32x4& rs, unsigned voff, unsigned so) {
  asm volatile("buffer_load_dwordx4 %0, %1, %2 offen lds" ::"v"(voff), "s"(rs), "s"(so) : "memory");
}
template <class T> DI void opq_s(T& v) { asm volatile("" : "+s"(v)); }
template <class T> DI void opq_v(T& v) { asm volatile("" : "+v"(v)); }
DI u32x4 cat4(const u32x2& a, const u32x2& b) { return u32x4{a[0], a[1], b[0], b[1]}; }

DI void a_mul(float& d, float a, float b) { asm volatile("v_mul_f32 %0, %1, %2" : "=v"(d) : "v"(a), "v"(b)); }
DI void a_exp(float& d) { asm volatile("v_exp_f32 %0, %0" : "+v"(d)); }
DI void a_rcp(float& d) { asm volatile("v_rcp_f32 %0, %0" : "+v"(d)); }
DI void a_fma11(float& d, float s) { asm volatile("v_fma_f32 %0, %0, %1, %1" : "+v"(d) : "v"(s)); }
DI void a_cvt(unsigned& d, float a, float b) { asm volatile("v_cvt_pk_bf16_f32 %0, %1, %2" : "=v"(d) : "v"(a), "v"(b)); }
DI void a_pl16(unsigned& a, unsigned& b) { asm volatile("v_permlane16_swap_b32 %0, %1" : "+v"(a), "+v"(b)); }

template <int OFF> DI void wr128(unsigned a, const f32x4& v) { asm volatile("ds_write_b128 %0, %1 offset:%2" ::"v"(a), "v"(v), "i"(OFF) : "memory"); }
template <int OFF> DI void rd128f(f32x4& d, unsigned a) { asm volatile("ds_read_b128 %0, %1 offset:%2" : "=v"(d) : "v"(a), "i"(OFF)); }
DI void a_add(float& d, float a) { asm volatile("v_add_f32 %0, %0, %1" : "+v"(d) : "v"(a)); }
template <int OFF> DI void gload16_c(u32x4& d, const void* sbase, unsigned voff) {
  asm volatile("global_load_dwordx4 %0, %1, %2 offset:%3 sc1" : "=v"(d) : "v"(voff), "s"(sbase), "i"(OFF) : "memory");
}
template <int OFF> DI void gload8_c(u32x2& d, const void* sbase, unsigned voff) {
  asm volatile("global_load_dwordx2 %0, %1, %2 offset:%3 sc1" : "=v"(d) : "v"(voff), "s"(sbase), "i"(OFF) : "memory");
}
template <int OFF> DI void gload4_c(unsigned& d, const void* sbase, unsigned voff) {
  asm volatile("global_load_dword %0, %1, %2 offset:%3 sc1" : "=v"(d) : "v"(voff), "s"(sbase), "i"(OFF) : "memory");
}
template <int OFF> DI void gload8(u32x2& d, const void* sbase, unsigned voff) {
  asm volatile("global_load_dwordx2 %0, %1, %2 offset:%3" : "=v"(d) : "v"(voff), "s"(sbase), "i"(OFF) : "memory");
}
template <int OFF> DI void gstore8_wt(void* sbase, unsigned voff, u32x2 v) {
  asm volatile("global_store_dwordx2 %0, %1, %2 offset:%3 sc1\n\ts_nop 1" ::"v"(voff), "v"(v), "s"(sbase), "i"(OFF) : "memory");
}
template <int V, int L> DI void wait_vl2() {
  if constexpr (V >= 63 && L >= 15) {}
  else if constexpr (V >= 63) asm volatile("s_waitcnt lgkmcnt(%0)" ::"i"(L) : "memory");
  else if constexpr (L >= 15) asm volatile("s_waitcnt vmcnt(%0)" ::"i"(V) : "memory");
  else asm volatile("s_waitcnt vmcnt(%0) lgkmcnt(%1)" ::"i"(V), "i"(L) : "memory");
}
DI unsigned umax3(unsigned a, unsigned b, unsigned c) { return __builtin_elementwise_max(__builtin_elementwise_max(a, b), c); }

DI int tb_j(int tb) { return tb >= 126 ? tb - 121 : tb % 7; }
DI int tb_first(int tb, int j) { return 16 * tb - (j && !(j & 1) ? 15 : 0); }
DI int tb_tok(int j, int l) { return j ? 2 * l : 2 * (l & 7) + (l >> 3); }
DI int tb_row(int j, int l) { return j ? (~j & 1) * 64 + ((j + 1) >> 1) * 16 + l : (l >> 3) * 64 + 8 + (l & 7); }

DI float dot2(unsigned a, unsigned b, float c) { return __builtin_amdgcn_fdot2_f32_bf16(__builtin_bit_cast(bf16x2, a), __builtin_bit_cast(bf16x2, b), c, false); }
template <int C> DI float dpp_add(float v) { return v + __builtin_bit_cast(float, __builtin_amdgcn_update_dpp(0, __builtin_bit_cast(int, v), C, 0xf, 0xf, false)); }
DI float sum8(float v) { return dpp_add<0x141>(dpp_add<0x4e>(dpp_add<0xb1>(v))); }
DI float bc0(float v) { return __builtin_bit_cast(float, __builtin_amdgcn_update_dpp(0, __builtin_bit_cast(int, v), 0x150, 0xf, 0xf, true)); }
DI float xmax4(float v) {
  auto r = __builtin_amdgcn_permlane32_swap(__float_as_uint(v), __float_as_uint(v), false, false); v = __builtin_fmaxf(__uint_as_float(r[0]), __uint_as_float(r[1]));
  auto q = __builtin_amdgcn_permlane16_swap(__float_as_uint(v), __float_as_uint(v), false, false); return __builtin_fmaxf(__uint_as_float(q[0]), __uint_as_float(q[1]));
}
DI f32x4 mfb(const u32x4& a, const u32x4& b, const f32x4& c) { return __builtin_amdgcn_mfma_f32_16x16x32_bf16(__builtin_bit_cast(bf16x8, a), __builtin_bit_cast(bf16x8, b), c, 0, 0, 0); }
template <int OFF> DI void wr128u(unsigned a, const u32x4& v) { asm volatile("ds_write_b128 %0, %1 offset:%2" ::"v"(a), "v"(v), "i"(OFF) : "memory"); }
template <int OFF> DI void wr64u(unsigned a, const u32x2& v) { asm volatile("ds_write_b64 %0, %1 offset:%2" ::"v"(a), "v"(v), "i"(OFF) : "memory"); }
template <int OFF> DI void rd64f(f32x2& d, unsigned a) { asm volatile("ds_read_b64 %0, %1 offset:%2" : "=v"(d) : "v"(a), "i"(OFF)); }
DI void vadd_s(unsigned& a, int s) { asm volatile("v_add_u32 %0, %1, %0" : "+v"(a) : "s"(s)); }
template <int LO> DI void glds(const void* sbase, unsigned voff, unsigned lds) {
  asm volatile("s_add_u32 m0, %2, %3\n\ts_nop 0\n\tglobal_load_lds_dwordx4 %0, %1" ::"v"(voff), "s"(sbase), "s"(lds), "i"(LO) : "memory", "scc");
}
DI void touch(const void* sbase, unsigned voff) { asm volatile("global_load_lds_dword %0, %1" ::"v"(voff), "s"(sbase) : "memory"); }
DI void gstore4_wt(void* sbase, unsigned voff, unsigned v) { asm volatile("global_store_dword %0, %1, %2 sc1" ::"v"(voff), "v"(v), "s"(sbase) : "memory"); }

namespace k4 {
#define K4_N0 6
#define K4_LA 24
#define K4_FAST 2
#define K4_UPL 150
constexpr int N0 = K4_N0, UA = 18 * N0, UB = 126, UE = 162, NR = 41, RR_ = N0 == 7 ? 13 : 14, RING = 8 * RR_, RBO = RING * 1024, RBS = N0 * 1024, LA = K4_LA, UPL = K4_UPL;
constexpr int slot_of(int u) { return u < UA ? u % N0 : u < UB ? 6 : 7 + ((u - UB) & 1); }
constexpr int ks_of(int u) { return u < UA ? u / N0 : u < UB ? u - UA : (u - UB) >> 1; }
}

namespace k1 {
constexpr int XB = 16384, SLOT = XB + 27648, EX = 3 * SLOT, ST = EX + 24576, VL = ST + 1024, PFD = VL + 4096, LDSZ = PFD + 256, RMO = 73728;
constexpr unsigned MG0 = 0x6b8f31d7u, MG1 = 0x1ce4a59bu;
#define K1RD 5
#define K1_N0 1
#define K1H 0
}
DI void k1_pf(const u16* x, const u16* Wq, int xcd, int g, int s, int lane) {
  const unsigned l = min(lane, 29), r = l >= 15, c = l - 15 * r;
  const unsigned ow = (g * 64 + s * 2 + r) * 3456 + ((c * 13) >> 6) * 512 + c * 128 + (xcd & 1) * 512;
  const int tok = min(max(((xcd >> 1) * 19 >> 2) * 112 - 16 + s * 22 + min(lane - 30, 21), 0), S_ - 1);
  const char* p = lane < 30 ? (const char*)Wq + ow : (const char*)x + (tok * (D_ * 2) + g * 128);
  asm volatile("global_load_lds_dword %0, off" ::"v"(p) : "memory");
}
DI void k1_wait(u32x2& f, const char* fb, unsigned fo) {
#pragma unroll 1
  for (int spin = 0; __builtin_amdgcn_ballot_w64(f[0] != k1::MG0 || f[1] != k1::MG1) != 0 && spin < (1 << 18); ++spin)
    asm volatile("s_sleep 1\n\tglobal_load_dwordx2 %0, %1, %2 sc1\n\ts_waitcnt vmcnt(0)" : "+v"(f) : "v"(fo), "s"(fb) : "memory");
}

__global__ __launch_bounds__(768, 3) void k1_attn(const u16* __restrict__ x, const u16* __restrict__ Wq, const u16* __restrict__ Wo, const u16* Wg, const u16* Wu, const u16* Wd, char* __restrict__ ws) {
  using namespace k1;
  __shared__ __attribute__((aligned(1024))) char smem[LDSZ];
  STAMP_BEGIN();
  const unsigned sb = (unsigned)(size_t)smem;
  const int w = __builtin_amdgcn_readfirstlane(threadIdx.x >> 6);
  const int lane = threadIdx.x & 63, g = lane >> 4, c = lane & 15;
  const int b = blockIdx.x, xcd = b & 7, bi = b >> 3;
  dma_setr(sb + PFD);
  if (w >= 4 && w < 4 + K1_N0) k1_pf(x, Wq, xcd, w - 4, bi, lane);
  if (w == 11) {
    u64 pc; unsigned len;
    asm volatile("s_getpc_b64 %0\n.Lpf_here:\n\ts_mov_b32 %1, (k_text1-.Lpf_here)&4294967295" : "=s"(pc), "=s"(len));
    touch((const void*)pc, min((unsigned)(bi + 32 * lane) * 128u, len - 4));
  }
  const int rrow = g * 4 + (c >> 2);
  if (bi >= 19) {
    const int hi = bi - 19, hw = hi + 13 * w;
    if (hi < 12) {
      for (int q = hi + 12 * w; q < 32 * (9 - K1_N0); q += 144) k1_pf(x, Wq, xcd, K1_N0 + (q >> 5), q & 31, lane);
      wait_vm<0>();
    } else {
      u32x4 v[9];
      const unsigned r = xcd * 72 + min(w, 8) * 8 + (lane >> 3);
      sfor<0, 9>([&](auto ic) AINL { constexpr int i = decltype(ic)::v; gload16<i * 128>(v[i], Wo, r * (D_ * 2) + (lane & 7) * 16); });
      asm volatile("s_waitcnt vmcnt(0)" : "+v"(v[0]), "+v"(v[1]), "+v"(v[2]), "+v"(v[3]), "+v"(v[4]), "+v"(v[5]), "+v"(v[6]), "+v"(v[7]), "+v"(v[8])::"memory");
      float t = 0.f;
      for (int i = 0; i < 36; ++i) t = dot2(v[i >> 2][i & 3], 0x3f803f80u, t);
      t = sum8(t) * (1.f / D_);
      const float hv = rbf(t);
      const unsigned hd = (r * 3641) >> 16, d = r - 18 * hd - 2 * (hd & 1), pos = d < 16 ? 16 * hd + d : 512 + 2 * (hd & 16) + 8 * (hd & 3) + ((hd >> 1) & 6) + (d & 1);
      if ((lane & 7) < 2 && w < 9) asm volatile("global_store_short %0, %1, %2 sc1" ::"v"((lane & 1) * (D_ * 2) + pos * 2), "v"(pk2((lane & 1) ? t - hv : hv, 0.f)), "s"(ws + WS_RM) : "memory");
      wait_vm<0>();
      abar();
      if (w < 3) gstore8_wt<0>(ws + WS_FR + xcd * 2048 + w * 512, lane * 8, u32x2{MG0, MG1});
    }
    STAMP_MID(1);
    if (hw < 81) touch((const char*)Wo + hw * 8192, lane * 128);
#pragma unroll 1
    for (int n = (hi * 8 + xcd) * 12 + w; n < 1296; n = w == 11 ? 1248 + (n < 1248 ? hi * 8 + xcd : 48) : 1296) {
      const int ksg = n / 9, cc = n - ksg * 9;
      const int q = ksg / 36, kk = ksg - q * 36, kh = kk >= 18, ks = kk - 18 * kh;
      const char* src = (const char*)Wd + ksg * 32 * (D_ * 2) + cc * 128;
      sfor<0, 4>([&](auto ic) AINL { constexpr int i = decltype(ic)::v;
        const unsigned p = i * 8 + (lane >> 3), kn = 8 * ((p >> 2) & 3) + 4 * (p >> 4) + (p & 3);
        glds<i * 1024>(src, (ks >= 16 ? kn : p) * (D_ * 2) + ((((lane >> 1) & 3) ^ ((p >> 1) & 3)) * 32) + (lane & 1) * 16, sb + w * 4096); });
      if (n < 1248) {
        const u32x4 ones = {~0u, ~0u, ~0u, ~0u};
        if (!(K1H & 1)) for (int e = hw; e < 32 * 27; e += 156) { const int blk = (e * 2428) >> 16; gstore16_wt<0>(ws + WS_PART + (size_t)(blk * 8 + xcd) * PART_BLK + (e - blk * 27) * 1024, lane * 16, ones); }
        dma_setr(sb + PFD);
        const unsigned id = min(lane, 53), r = (id * 3641) >> 16, o = r * (F_ * 2 - 2304) + id * 128 + (xcd & 3) * 2304;
        for (int e = hw; e < 192; e += 156) touch((const char*)(xcd & 4 ? Wu : Wg) + (size_t)e * (3 * F_ * 2), o);
        wait_vm<(K1H & 1) ? 1 : 6>();
      } else wait_vm<0>();
      u32x2 v[4][2];
      sfor<0, 4>([&](auto tc) AINL { constexpr int t = decltype(tc)::v;
        const unsigned a = sb + w * 4096 + rrow * 128 + ((t ^ ((rrow >> 1) & 3)) * 32) + (lane & 3) * 8; trd<0>(v[t][0], a); trd<2048>(v[t][1], a); });
      sfor<0, 4>([&](auto tc) AINL { constexpr int t = decltype(tc)::v;
        const unsigned T = cc * 4 + t, cg = T / 9, lt = T - cg * 9 - (cg & 1), sl = (((lt >> 1) - q - 1) & 3) * 2 + (lt & 1);
        const unsigned u = lt >= 8 ? (k4::N0 == 7 ? ks * 7 + 6 : k4::UA + ks) : sl < 6 ? ks * k4::N0 + sl : k4::UB + 2 * ks + sl - 6;
        asm volatile("s_waitcnt lgkmcnt(%2)" : "+v"(v[t][0]), "+v"(v[t][1]) : "i"(6 - 2 * t));
        gstore16_wt<0>(ws + WS_WDP + (size_t)((q * 4 + cg) * 324 + 2 * u + kh) * 1024, lane * 16, cat4(v[t][0], v[t][1])); });
    }
    wait_vm<0>();
    STAMP_END(ws, 0);
    return;
  }
  const int u = (xcd >> 1) * 19 + bi, tile = u >> 2, hg = (xcd & 1) * 4 + (u & 3), T0 = min(112 * tile, 1936);
  const int cg = bi >= 9 ? min(bi - 9, 8) : bi, rot = (cg * 7) >> 2;
  char* ol = ws + WS_OL + (tile == 18 ? 121 : 7 * tile) * OLB;
  if (w < 4) {
    __builtin_amdgcn_s_setprio(3);
    unsigned vx[4], vw[7];
    const char* xs = (const char*)x; const char* wsrc = (const char*)Wq + hg * 144;
    unsigned dx = sb + w * 4096, dw = sb + XB + w * 7168, dl = dw - (w == 3 ? 1024 : 0);
    const u64 em = (w & 1) ? ~0ull : ~0xffull;
    int step = SLOT;
    u32x4 one = {0x3f803f80u, 0x3f803f80u, 0x3f803f80u, 0x3f803f80u}, X[4] = {};
    f32x4 as[2] = {}, aq[2] = {};
    auto stat = [&](auto ic) AINL { constexpr int i = decltype(ic)::v;
      if constexpr (i & 1) mf(aq[(i >> 1) & 1], X[i >> 1], X[i >> 1]); else mf(as[(i >> 1) & 1], one, X[i >> 1]); };
    unsigned sa, sa2;
    int sl = 0;
    auto rdx = [&]() AINL {
      rd128<0>(X[0], sa); rd128<2048>(X[1], sa); rd128<0>(X[2], sa2); rd128<2048>(X[3], sa2);
      const int st = sl == 2 ? -2 * SLOT : SLOT;
      vadd_s(sa, st); vadd_s(sa2, st);
      sl = sl == 2 ? 0 : sl + 1; };
    auto issue = [&](auto fc) AINL {
      sfor<0, 11>([&](auto ic) AINL { constexpr int i = decltype(ic)::v;
        if constexpr (decltype(fc)::v == 1 && i < 4) { const int r = (w & 1) * 32 + 8 * i + (lane >> 3);
          vx[i] = max(T0 - 16 + (w >> 1) + 2 * r, 0) * (D_ * 2) + ((lane ^ (r >> 1)) & 7) * 16; opq_v(vx[i]); }
        if constexpr (decltype(fc)::v == 1 && i >= 4) { const unsigned L = min(w * 7 + i - 4, 26) * 64 + lane, r = (L * 2428) >> 16, cc = L - r * 27;
          vw[i - 4] = ((r & 56) + (r & 1) * 4 + ((r >> 1) & 3)) * 3456 + ((cc * 57) >> 9) * 1008 + cc * 16; opq_v(vw[i - 4]); }
        if constexpr (i == 0) asm volatile("s_mov_b64 exec, %3\n\ts_mov_b32 m0, %2\n\ts_nop 0\n\tglobal_load_lds_dwordx4 %0, %1\n\ts_mov_b64 exec, -1" ::"v"(vx[0]), "s"(xs), "s"(dx), "s"(em) : "memory");
        else if constexpr (i < 4) glds<i * 1024>(xs, vx[i], dx);
        else if constexpr (i < 10) glds<(i - 4) * 1024>(wsrc, vw[i - 4], dw);
        else glds<6144>(wsrc, vw[6], dl);
        if constexpr (decltype(fc)::v == 2 && i < 4) { stat(IC<2 * i>{}); stat(IC<2 * i + 1>{}); }
        if constexpr (decltype(fc)::v == 2 && i == K1RD) rdx(); });
      xs += 128; wsrc += 64 * 3456; dx += step; dw += step; dl += step;
    };
    issue(IC<1>{});
    if (!(w & 1) && lane < 24) *LDSP(u32x4, sb + (lane >> 3) * SLOT + w * 4096 + (lane & 7) * 16) = u32x4{0x3f803f80u, 0x3f803f80u, 0x3f803f80u, 0x3f803f80u};
    issue(IC<0>{});
    sa = sb + (32 * w + c) * 128 + (g ^ (c >> 1)) * 16; sa2 = sa ^ 64;
    unsigned v0, v1, fo = (lane & 7) * 2048 + b * 8, ro;
    { const int p = w * 8 + (lane >> 3), gq = (p >> 2) & 3, e = 4 * (p >> 4) + (p & 3), hd = 4 * (e >> 1) + gq;
      const unsigned co = ((((lane >> 1) & 3) ^ ((p >> 1) & 3)) * 32) + (lane & 1) * 16 + cg * 128;
      v0 = (10 * (gq & 2) + 8 * (gq & 1) + e) * (D_ * 2) + co; v1 = (18 * hd + (~hd & 1) * 16 + (e & 1)) * (D_ * 2) + co;
      const unsigned q = (w & 1) * 36 + min(lane, 35);
      ro = (w >> 1) * (D_ * 2) + (q < 64 ? (q + 4 * rot) & 63 : q) * 16; }
    opq_v(one); opq_v(as[0]); opq_v(as[1]); opq_v(aq[0]); opq_v(aq[1]); opq_v(v0); opq_v(v1); opq_v(fo); opq_v(ro);
    auto wo = [&](auto ic) AINL { constexpr int i = decltype(ic)::v;
      glds<i * 4096>((const char*)Wo + (i < 16 ? ((i + rot) & 15) * 36 : 288 * (i - 16)) * (D_ * 2), i < 16 ? v0 : v1, sb + w * 1024); };
#pragma unroll 1
    for (int it = 0; it < 7; ++it) {
      asm volatile("s_waitcnt vmcnt(11) lgkmcnt(0)" : "+v"(X[0]), "+v"(X[1]), "+v"(X[2]), "+v"(X[3])::"memory");
      abar();
      step = sl == 0 ? -2 * SLOT : SLOT;
      issue(IC<2>{});
    }
    asm volatile("s_waitcnt vmcnt(11) lgkmcnt(0)" : "+v"(X[0]), "+v"(X[1]), "+v"(X[2]), "+v"(X[3])::"memory");
    abar();
    u32x2 fl; u32x4 rmv;
    gload8_c<0>(fl, ws + WS_FR, fo);
    sfor<0, 10>([&](auto ic) AINL { constexpr int i = decltype(ic)::v; wo(ic);
      if constexpr (i < 4) { stat(IC<2 * i>{}); stat(IC<2 * i + 1>{}); }
      if constexpr (i == 4) rdx(); });
    asm volatile("s_waitcnt vmcnt(10) lgkmcnt(0)" : "+v"(X[0]), "+v"(X[1]), "+v"(X[2]), "+v"(X[3]), "+v"(fl)::"memory");
    abar();
    sfor<0, 8>(stat);
    rdx();
    k1_wait(fl, ws + WS_FR, fo);
    gload16_c<0>(rmv, ws + WS_RM, ro);
    asm volatile("s_waitcnt lgkmcnt(0)" : "+v"(X[0]), "+v"(X[1]), "+v"(X[2]), "+v"(X[3])::"memory");
    sfor<0, 8>(stat);
    asm volatile("s_nop 7\n\ts_nop 3" : "+v"(as[0]), "+v"(as[1]), "+v"(aq[0]), "+v"(aq[1]));
    for (int i = 0; i < 2; ++i) {
      const float m = as[i][0] * (1.f / D_), q = (c & 2) ? ((c & 1) ? aq[i][3] : aq[i][2]) : ((c & 1) ? aq[i][1] : aq[i][0]);
      const float rs = __builtin_amdgcn_rsqf(__builtin_fmaxf(__builtin_fmaf(-m, m, q * (1.f / D_)), 0.f) + 1e-5f);
      if (g == c >> 2) *LDSP(f32x2, sb + ST + (32 * w + 16 * i + c) * 8) = f32x2{-m, rs};
    }
    wait_vl2<63, 0>();
    abar();
    sfor<10, 18>(wo);
    { const int n = 2 * (w & 1) + g;
      if (hg == 0 && g < 2 && (tile == 18 ? n == 3 : n || c >= 8))
        gstore4_wt(ol + 2048, (n ? (2 * n - 1 + (w >> 1)) * OLB : (w >> 1) * 32 - 32) + c * 4, __float_as_uint((g ? as[1][0] : as[0][0]) * (1.f / D_))); }
    asm volatile("s_waitcnt vmcnt(0)" : "+v"(rmv)::"memory");
    if (lane < 36) *LDSP(u32x4, sb + RMO + w * 576 + lane * 16) = rmv;
    wait_vl2<63, 0>();
    abar();
    if (w == 0) gstore8_wt<0>(ws + WS_FL + (tile * 8 + hg) * 512, lane * 8, u32x2{MG0, MG1});
    if (w == 1 && lane < 8) gstore8_wt<0>(ws + WS_FR, fo, u32x2{0u, 0u});
    return;
  }
  {
  const int h = w & 3, par = (w >> 2) - 1, f = h & 1;
  unsigned xa = sb + (par * 64 + c) * 128 + ((g ^ (c >> 1)) * 16 ^ f * 64), xb = xa ^ 64;
  const unsigned wr = sb + XB + (8 * g + 2 * (c >> 2)) * 432;
  unsigned wa = wr + (c & 3) * 8 + 36 * h + 4 * f + f * 13824, wb = wa + 13824 - f * 27648;
  unsigned la = wr + f * 13824 + 32 + 72 * (h >> 1) + ((c & 2) ? 288 : (c & 1) ? 0 : 144);
  unsigned ea = sb + EX + (w - 4) * 3072 + min(g, 2) * 256 + c * 16;
  f32x4 acc[4][4], bias[3];
  unsigned mk[2];
  for (int i = 0; i < 16; ++i) { acc[i >> 2][i & 3] = f32x4{0.f, 0.f, 0.f, 0.f}; opq_v(acc[i >> 2][i & 3]); }
  for (int r = 0; r < 4; ++r) { const int d = c - 4 * g - r; const float ninf = -__builtin_inff();
    bias[1][r] = d >= 0 && d < 8 ? 0.f : ninf; bias[2][r] = d < -8 ? 0.f : ninf; bias[0][r] = (T0 == 0 && g < 2) ? ninf : bias[1][r]; }
  for (int e = 0; e < 2; ++e) { const int d = c - 4 * g - 2 * e; mk[e] = d > 0 ? ~0u : d == 0 ? 0xffffu : 0u; opq_v(mk[e]); }
  opq_v(bias[0]); opq_v(bias[1]); opq_v(bias[2]); opq_v(xa); opq_v(xb); opq_v(wa); opq_v(wb); opq_v(la); opq_v(ea);
  u32x4 X0[4], X1[4];
  u32x2 W0[4][2], W1[4][2];
  auto rd0 = [&](auto ic) AINL { constexpr int i = decltype(ic)::v;
    if constexpr (i == 0) rd128<0>(X0[0], xa);
    else if constexpr (i < 3) trd<(i - 1) * 432>(W0[0][i - 1], la);
    else if constexpr (i < 6) rd128<(i - 2) * 2048>(X0[i - 2], xa);
    else trd<((i - 6) >> 1) * 144 + (i & 1) * 432>(W0[1 + ((i - 6) >> 1)][i & 1], wa); };
  auto rd1 = [&](auto ic) AINL { constexpr int i = decltype(ic)::v;
    if constexpr (i < 4) rd128<i * 2048>(X1[i], xb);
    else trd<((i - 4) >> 1) * 144 + (i & 1) * 432>(W1[1 + ((i - 4) >> 1)][i & 1], wb); };
  auto mfB = [&](auto mc) AINL { constexpr int t = 1 + decltype(mc)::v / 4, n = decltype(mc)::v & 3;
    if constexpr (t < 3) mf(acc[t][n], cat4(W1[t][0], W1[t][1]), X1[n]); else mf(acc[t][n], X1[n], cat4(W1[t][0], W1[t][1])); };
  int sl = 0;
  auto phA = [&](auto lc) AINL {
    const int st = sl == 2 ? -2 * SLOT : SLOT;
    sl = sl == 2 ? 0 : sl + 1;
    sfor<0, 16>([&](auto mc) AINL { constexpr int m = decltype(mc)::v, t = m >> 2, n = m & 3;
      if constexpr (m < 4) wait_vl2<63, 7>();
      if constexpr (m == 4) wait_vl2<63, 6>();
      if constexpr (m == 8) wait_vl2<63, 8>();
      if constexpr (m == 12) wait_vl2<63, 10>();
      if constexpr (t < 3) mf(acc[t][n], cat4(W0[t][0], W0[t][1]), X0[n]); else mf(acc[t][n], X0[n], cat4(W0[t][0], W0[t][1]));
      if constexpr (m < 2) rd0(IC<10 + m>{});
      if constexpr (m >= 2 && m < 12) rd1(IC<m - 2>{});
      if constexpr (!decltype(lc)::v) {
        if constexpr (m == 12) { vadd_s(xa, st); vadd_s(la, st); }
        if constexpr (m == 13) { vadd_s(wa, st); vadd_s(xb, st); }
        if constexpr (m == 14) vadd_s(wb, st);
      } else if constexpr (m >= 12) wr128<(m - 12) * 768>(ea, acc[0][m - 12]);
    });
    asm volatile("s_waitcnt lgkmcnt(0)" : "+v"(X1[0]), "+v"(X1[1]), "+v"(X1[2]), "+v"(X1[3]), "+v"(W1[1][0]), "+v"(W1[1][1]), "+v"(W1[2][0]), "+v"(W1[2][1]), "+v"(W1[3][0]), "+v"(W1[3][1])::"memory");
    abar();
  };
  abar();
  STAMP_MID(1);
  sfor<0, 10>(rd0);
#pragma unroll 1
  for (int it = 0; it < 8; ++it) {
    phA(IC<0>{});
    sfor<0, 12>([&](auto mc) AINL { constexpr int m = decltype(mc)::v; mfB(mc); if constexpr (m < 10) rd0(mc); });
  }
  phA(IC<1>{});
  STAMP_MID(2);
  f32x2 sT[4], lf[4][2];
  f32x4 sR[4][2];
  { unsigned e0 = ea + f * 8, e1 = e0 + (f ? -3072 : 3072), s0 = sb + ST + (par * 64 + c) * 8, s1 = sb + ST + (par * 64 + 4 * g) * 8;
    opq_v(e0); opq_v(e1); opq_v(s0); opq_v(s1);
    sfor<0, 12>([&](auto mc) AINL { constexpr int m = decltype(mc)::v; mfB(mc);
      if constexpr (m < 4) { rd64f<m * 128>(sT[m], s0); rd64f<m * 768>(lf[m][0], e0); rd64f<m * 768>(lf[m][1], e1); }
      else if constexpr (m < 8) { rd128f<(m - 4) * 128>(sR[m - 4][0], s1); rd128f<(m - 4) * 128 + 16>(sR[m - 4][1], s1); } }); }
  asm volatile("s_waitcnt lgkmcnt(0)" : "+v"(sT[0]), "+v"(sT[1]), "+v"(sT[2]), "+v"(sT[3]), "+v"(lf[0][0]), "+v"(lf[0][1]), "+v"(lf[1][0]), "+v"(lf[1][1]), "+v"(lf[2][0]), "+v"(lf[2][1]), "+v"(lf[3][0]), "+v"(lf[3][1]),
               "+v"(sR[0][0]), "+v"(sR[0][1]), "+v"(sR[1][0]), "+v"(sR[1][1]), "+v"(sR[2][0]), "+v"(sR[2][1]), "+v"(sR[3][0]), "+v"(sR[3][1]), "+v"(acc[1][0]), "+v"(acc[1][1]), "+v"(acc[1][2]), "+v"(acc[1][3])::"memory");
  u32x4 Kf[4], Qf[4];
  const unsigned vl = sb + VL + (w - 4) * 512;
  { float cs[2]; f32x2 l[4];
    for (int n = 0; n < 4; ++n) l[n] = lf[n][0] + lf[n][1];
    for (int e = 0; e < 2; ++e) cs[e] = bc0(l[0][e]);
    for (int n = 0; n < 4; ++n) { const float fc = (g & 1) ? (g == 1 ? 1.f : 0.f) : sT[n][1];
      const unsigned p = pk2(__builtin_fmaf(sT[n][0], cs[0], l[n][0]) * fc, __builtin_fmaf(sT[n][0], cs[1], l[n][1]) * fc);
      if (g == 2) *LDSP(u32x2, vl + (16 * n + c) * 8) = u32x2{p, 0x3f803f80u};
      auto r = __builtin_amdgcn_permlane16_swap(p, 0u, false, false);
      Kf[n][2] = r[0]; Qf[n][2] = r[1]; Kf[n][3] = 0u; Qf[n][3] = 0u; } }
  u32x2 VLo[6];
  { unsigned a = vl + (4 * g + (c >> 2)) * 8; asm volatile("s_waitcnt lgkmcnt(0)" : "+v"(a)::"memory");
    trd<0>(VLo[0], a); trd<128>(VLo[1], a); trd<128>(VLo[2], a); trd<256>(VLo[3], a); trd<256>(VLo[4], a); trd<384>(VLo[5], a); }
  for (int n = 3; n >= 0; --n) for (int e = 0; e < 2; ++e)
    Qf[n][e] = pk2(__builtin_fmaf(sT[n][0], bc0(acc[1][0][2 * e]), acc[1][n][2 * e]), __builtin_fmaf(sT[n][0], bc0(acc[1][0][2 * e + 1]), acc[1][n][2 * e + 1]));
  asm volatile("" : "+v"(acc[2][0]), "+v"(acc[2][1]), "+v"(acc[2][2]), "+v"(acc[2][3]), "+v"(Qf[0]));
  for (int n = 3; n >= 0; --n) for (int e = 0; e < 2; ++e)
    Kf[n][e] = pk2(__builtin_fmaf(sT[n][0], bc0(acc[2][0][2 * e]), acc[2][n][2 * e]) * sT[n][1], __builtin_fmaf(sT[n][0], bc0(acc[2][0][2 * e + 1]), acc[2][n][2 * e + 1]) * sT[n][1]);
  f32x4 S[4];
  for (int n = 0; n < 4; ++n) {
    S[n] = mfb(Kf[n], Qf[n], bias[n > 0]);
    if (n) S[n] = __builtin_elementwise_max(S[n], mfb(Kf[n - 1], Qf[n], bias[2]));
  }
  asm volatile("" : "+v"(acc[3][0]), "+v"(acc[3][1]), "+v"(acc[3][2]), "+v"(acc[3][3]), "+v"(Kf[0]));
  u32x2 Vf[4];
  { auto r = __builtin_amdgcn_permlane16_swap(__float_as_uint(acc[3][0][0]), __float_as_uint(acc[3][0][0]), false, false);
    auto q = __builtin_amdgcn_permlane32_swap(r[0], r[0], false, false);
    const float cs = __uint_as_float(q[0]);
    for (int n = 0; n < 4; ++n) for (int e = 0; e < 2; ++e)
      Vf[n][e] = pk2(__builtin_fmaf(sR[n][e][0], cs, acc[3][n][2 * e]) * sR[n][e][1], __builtin_fmaf(sR[n][e][2], cs, acc[3][n][2 * e + 1]) * sR[n][e][3]); }
  asm volatile("s_waitcnt lgkmcnt(0)" : "+v"(VLo[0]), "+v"(VLo[1]), "+v"(VLo[2]), "+v"(VLo[3]), "+v"(VLo[4]), "+v"(VLo[5])::"memory");
  u32x2 om[4]; unsigned olf[4];
  for (int n = 0; n < 4; ++n) {
    const float mx = xmax4(__builtin_fmaxf(__builtin_fmaxf(S[n][0], S[n][1]), __builtin_fmaxf(S[n][2], S[n][3])));
    const float a = sT[n][1] * (0.23570226039551584f * 1.44269504088896f), ma = -mx * a;
    float p[4];
    for (int r = 0; r < 4; ++r) p[r] = __builtin_amdgcn_exp2f(__builtin_fmaf(S[n][r], a, ma));
    const unsigned p0 = pk2(p[0], p[1]), p1 = pk2(p[2], p[3]);
    const u32x4 P = n ? u32x4{p0 & ~mk[0], p1 & ~mk[1], p0 & mk[0], p1 & mk[1]} : u32x4{p0, p1, 0u, 0u};
    const int n0 = n ? n - 1 : 0;
    const f32x4 z = {0.f, 0.f, 0.f, 0.f}, o = mfb(cat4(Vf[n0], Vf[n0 + 1]), P, z), l = mfb(cat4(VLo[2 * n0], VLo[2 * n0 + 1]), P, z);
    const float inv = __builtin_amdgcn_rcpf(l[3]);
    om[n] = u32x2{pk2(o[0] * inv, o[1] * inv), pk2(o[2] * inv, o[3] * inv)}; olf[n] = pk2(l[0] * inv, l[1] * inv);
  }
  { char* ob = ws + WS_OF + (size_t)(tile == 18 ? 121 : 7 * tile) * 16384 + (hg * 4 + h) * 512;
    const unsigned o0 = (g >> 1) * 256 + c * 16;
    for (int i = 1; i >= 0; --i) {
      auto r = __builtin_amdgcn_permlane16_swap(om[2 * i][0], om[2 * i + 1][0], false, false), q = __builtin_amdgcn_permlane16_swap(om[2 * i][1], om[2 * i + 1][1], false, false);
      const int n = 2 * i + (g & 1);
      if (tile == 18 ? n == 3 : n || c >= 8) gstore16_wt<0>(ob, n ? o0 + (2 * n - 1 + par) * 16384 : o0 + par * 128 - 128, u32x4{r[0], q[0], r[1], q[1]});
    }
    if (tile == 18 ? g == 3 : g || c >= 8)
      gstore4_wt(ol + (hg * 4 + h) * 64, (g ? (2 * g - 1 + par) * OLB : par * 32 - 32) + c * 4, (g & 2) ? ((g & 1) ? olf[3] : olf[2]) : ((g & 1) ? olf[1] : olf[0])); }
  }
  STAMP_MID(3);
  const int tb = (2 * xcd + (bi >= 9)) * 8 + w - 4, j = tb_j(tb);
  u32x4 X[18], xr[2];
  unsigned mx, XL[8];
  { const char* xb = (const char*)x + (size_t)tb_first(tb, j) * (D_ * 2) + cg * 128; const unsigned o = tb_tok(j, c) * (D_ * 2) + g * 16;
    gload16<0>(xr[0], xb, o); gload16<64>(xr[1], xb, o); }
  unsigned ra[4];
  for (int t = 0; t < 4; ++t) ra[t] = sb + rrow * 128 + ((t ^ ((rrow >> 1) & 3)) * 32) + (lane & 3) * 8;
  unsigned rm = sb + RMO + (lane & 1) * (D_ * 2) + g * 16;
  opq_v(ra[0]); opq_v(ra[1]); opq_v(ra[2]); opq_v(ra[3]); opq_v(rm);
  u32x4 s0, s1;
  { const unsigned one = 0x3f80u << (16 * (lane & 1)); const int d = (lane & 7) >> 1, hit = g - ((lane >> 3) & 1);
    for (int i = 0; i < 4; ++i) { s0[i] = (i == d && hit == 0) ? one : 0u; s1[i] = (i == d && hit == 2) ? one : 0u; }
    opq_v(s0); opq_v(s1); }
  char* fb = ws + WS_FL + (tb / 7) * 4096 + (j * 9 + cg) * 8;
  const char* ob = ws + WS_OF + (size_t)tb * 16384; const char* lb = ws + WS_OL + tb * OLB;
  opq_s(fb); opq_s(ob); opq_s(lb);
  f32x4 acc[5];
  asm volatile("s_waitcnt vmcnt(0)" : "+v"(xr[0]), "+v"(xr[1])::"memory");
  abar();
  STAMP_MID(4);
  if (bi == 18) { STAMP_END(ws, 0); return; }
  for (int t = 0; t < 4; ++t) mf0(acc[t], (t & 1) ? s1 : s0, xr[t >> 1]);
  { u32x2 fl = {0u, 0u}; k1_wait(fl, fb, (lane & 7) * 512); }
  STAMP_MID(5);
  {
    sfor<0, 16>([&](auto kc) AINL { constexpr int k = decltype(kc)::v; gload16_c<0>(X[k], ob + ((k + rot) & 15) * 1024, lane * 16); }); }
  { sfor<0, 8>([&](auto kc) AINL { constexpr int k = decltype(kc)::v; gload4_c<k * 256>(XL[k], lb, lane * 4); });
    gload4_c<2048>(mx, lb, c * 4); }
  if (lane < 8) gstore8_wt<0>(fb, lane * 512, u32x2{0u, 0u});
  u32x2 A[2][4][2];
  u32x4 Ar[2];
  sfor<0, 19>([&](auto kc) AINL {
    constexpr int k = decltype(kc)::v;
    if constexpr (k == 16) for (int t = 0; t < 4; ++t) vadd_opq<65536>(ra[t]);
    if constexpr (k >= 17) { constexpr int o = 4 * (k - 17);
      asm volatile("s_waitcnt vmcnt(%4)" : "+v"(XL[o]), "+v"(XL[o + 1]), "+v"(XL[o + 2]), "+v"(XL[o + 3]) : "i"(6 - o) : "memory");
      X[k - 1] = u32x4{XL[o], XL[o + 1], XL[o + 2], XL[o + 3]}; asm volatile("s_nop 1" : "+v"(X[k - 1])); }
    sfor<0, 5>([&](auto tc) AINL {
      constexpr int t = decltype(tc)::v;
      if constexpr (k < 18 && t < 4) { trd<(k & 15) * 4096>(A[k & 1][t][0], ra[t]); trd<(k & 15) * 4096 + 2048>(A[k & 1][t][1], ra[t]); }
      if constexpr (k < 18 && t == 4) rd128<k * 64>(Ar[k & 1], rm);
      if constexpr (k > 0) {
        wait_vl2<(t == 0 && k < 17 ? 26 - k : 63), (k < 18 ? 9 : t < 4 ? 7 - 2 * t : 0)>();
        if constexpr (t < 4) mf(acc[t], cat4(A[~k & 1][t][0], A[~k & 1][t][1]), X[k - 1]);
        else { if constexpr (k == 1) mf0(acc[4], Ar[~k & 1], X[0]); else mf(acc[4], Ar[~k & 1], X[k - 1]); }
      }
    });
  });
  STAMP_MID(6);
  asm volatile("s_waitcnt vmcnt(1)\n\ts_nop 7\n\ts_nop 3" : "+v"(mx), "+v"(acc[0]), "+v"(acc[1]), "+v"(acc[2]), "+v"(acc[3]), "+v"(acc[4])::"memory");
  const float mean = __uint_as_float(mx) + (acc[4][0] + acc[4][1]), mb = rbf(mean);
  u32x4 hf[2], hc[2];
  float ss = 0.f;
  for (int t = 0; t < 4; ++t)
    for (int i = 0; i < 2; ++i) {
      const unsigned p = pk2(acc[t][2 * i], acc[t][2 * i + 1]);
      const float a = lo16(p), e = hi16(p);
      hf[t >> 1][(t & 1) * 2 + i] = p;
      hc[t >> 1][(t & 1) * 2 + i] = pk2(a - mb, e - mb);
      ss = __builtin_fmaf(a - mean, a - mean, ss); ss = __builtin_fmaf(e - mean, e - mean, ss);
    }
  { auto r = __builtin_amdgcn_permlane32_swap(__float_as_uint(ss), __float_as_uint(ss), false, false); ss = __uint_as_float(r[0]) + __uint_as_float(r[1]);
    auto q = __builtin_amdgcn_permlane16_swap(__float_as_uint(ss), __float_as_uint(ss), false, false); ss = __uint_as_float(q[0]) + __uint_as_float(q[1]); }
  const size_t fo = ((size_t)tb * 18 + cg * 2) * 1024;
  gstore16_wt<0>(ws + WS_HC + fo, lane * 16, hc[0]); gstore16_wt<1024>(ws + WS_HC + fo, lane * 16, hc[1]);
  if (lane < 16) asm volatile("global_store_dword %0, %1, %2 sc1" ::"v"(lane * 4), "v"(ss), "s"(ws + WS_SSQ + cg * 8192 + tb * 64) : "memory");
  gstore16_wt<0>(ws + WS_HF + fo, lane * 16, hf[0]); gstore16_wt<1024>(ws + WS_HF + fo, lane * 16, hf[1]);
  STAMP_END(ws, 0);
}

#define K3_SKIP 0
namespace k3 {
constexpr int NSLOT = 7, C2OFF = NSLOT * 16384;
constexpr int U1 = 144, U2 = 180, U3 = 252, UE = 324;
enum { OW = 0, OX = 1, OC2 = 2, OSQ = 3, OST = 4, OPD = 5, OPS = 6, OFL = 7 };
enum { VM1 = 0, VEX, VFM, VRC, VM2, VCV, VPL };
struct Sched {
  int n; int Q[160]; int kind[160]; int idx[160];
  int nv; int VQ[400]; int vop[400]; int vsl[400]; int vix[400];
  int stq[7];
  constexpr void add(int q, int k, int i) { Q[n] = q; kind[n] = k; idx[n] = i; ++n; }
  static constexpr int ubar(int j) { return j <= 10 ? 16 * j + 13 : 177 + 16 * (j - 10); }
  static constexpr int uw(int L, int h) {
    if (L <= 8) return 16 * (L - 2) + 2 * h;
    if (L <= 13) return 140 + 8 * (L - 9) + 2 * h;
    return 186 + 16 * (L - 14) + 2 * h;
  }
  static constexpr int uc2(int i) { return i == 0 ? 100 : i == 1 ? 110 : i == 2 ? 118 : i == 3 ? 126 : 134; }
  constexpr int glu(int q, int a, int b, int cap) {
    int used = 0;
    for (int op = VM1; op <= VCV; ++op)
      for (int t = 0; t < (b >= 0 ? 2 : 1); ++t)
        for (int v = 0; v < (op == VCV ? 4 : 8); ++v) {
          int c = (op == VEX || op == VRC) ? 8 : 4;
          if (used && used + c > cap) { ++q; used = 0; }
          VQ[nv] = q; vop[nv] = op; vsl[nv] = t ? b : a; vix[nv] = v; ++nv; used += c;
        }
    return q + 1;
  }
  constexpr Sched() : n(0), Q{}, kind{}, idx{}, nv(0), VQ{}, vop{}, vsl{}, vix{}, stq{} {
    int q = glu(2 * U1 + 2, 0, 1, 8);
    stq[0] = (q + 1) & ~1; stq[1] = stq[0] + 4;
    q = glu(2 * U2 + 2, 2, 3, 4);
    stq[2] = (q + 1) & ~1; stq[3] = stq[2] + 16;
    q = glu(q, 4, -1, 4);
    for (int v = 0; v < 2; ++v) { VQ[nv] = q + 1 + v; vop[nv] = VPL; vsl[nv] = 4; vix[nv] = v; ++nv; }
    stq[4] = (q + 5) & ~1;
    q = glu(2 * U3 + 8, 0, 1, 4);
    stq[5] = (q + 1) & ~1; stq[6] = stq[5] + 16;
    add(-1, OW, 0); add(-1, OW, 1);
    for (int k = 0; k < 4; ++k) add(-1, OX, k);
    for (int i = 0; i < 5; ++i) add(-1, OSQ, i);
    add(-1, OW, 2); add(-1, OW, 3);
    for (int k = 4; k < 8; ++k) add(-1, OX, k);
    for (int qq = 0; qq < 2 * UE; ++qq) {
      int u = qq >> 1, h = qq & 1;
      if (h == 1) for (int L = 2; L < 18; ++L) for (int p = 0; p < 2; ++p) if (uw(L, p) == u) add(qq, OW, 2 * L + p);
      if (h == 1) for (int i = 0; i < 5; ++i) if (uc2(i) == u) add(qq, OC2, i);
      if (h == 0 && u < 16 * 7) { int r = u & 15; if (r == 5 || r == 7 || r == 9 || r == 11) add(qq, OX, 4 * ((u >> 4) + 2) + (r - 5) / 2); }
      for (int t = 0; t < 7; ++t) if (stq[t] == qq) add(qq, OST, t);
    }
  }
  constexpr int find(int k, int i) const { for (int j = 0; j < n; ++j) if (kind[j] == k && idx[j] == i) return j; return -1000; }
  constexpr int cnt(int q, int k, int i) const { int j = find(k, i), c = 0; for (int m = j + 1; m < n; ++m) if (Q[m] < q) ++c; return c; }
  constexpr int barat(int u) const { for (int j = 0; j < 19; ++j) if (ubar(j) == u) return j; return -1; }
  constexpr int vmat(int u) const {
    int v = 63;
    if (u < U1 && (u & 7) == 0) v = cnt(2 * u, OX, 2 * (u >> 3) + 1);
    int j = barat(u);
    if (j >= 0) { int c = j < 8 ? cnt(2 * u, OW, 2 * (j + 1) + 1) : j == 8 ? cnt(2 * u, OC2, 1) : j == 9 ? cnt(2 * u, OC2, 3) : cnt(2 * u, OW, 2 * (j - 1) + 1); if (c < v) v = c; }
    return v;
  }
  constexpr int opat(int q, int k) const { for (int j = 0; j < n; ++j) if (Q[j] == q && kind[j] == k) return idx[j]; return -1; }
  constexpr int vfirst(int q) const { for (int j = 0; j < nv; ++j) if (VQ[j] >= q) return j; return nv; }
};
constexpr Sched SC{};
static_assert(SC.stq[1] < 2 * U2 && SC.stq[4] < 2 * U3 + 8 && SC.stq[6] < 2 * UE, "schedule");
}

__global__ __launch_bounds__(512, 2) void k3_gateup(const u16* __restrict__ Wg, const u16* __restrict__ Wu, char* __restrict__ ws) {
  using namespace k3;
  __shared__ __attribute__((aligned(1024))) char smem[C2OFF + 36864];
  STAMP_BEGIN();
  const unsigned sb = (unsigned)(size_t)smem;
  const int w = __builtin_amdgcn_readfirstlane(threadIdx.x >> 6);
  const int lane = threadIdx.x & 63;
  const int b = blockIdx.x, xcd = b & 7, bi = b >> 3;
  const int tt = (xcd >> 2) * 4 + (bi >> 3);
  const int fg = (xcd & 3) * 8 + (bi & 7);
  const int G = fg >> 2, j = fg & 3;
  const int tb = tt * 16 + 2 * w;
  const int gu = w >> 2;
  const u32x4 rsW = mkrsrc(gu ? Wu : Wg, D_ * F_ * 2);
  unsigned wlo, w2lo;
  { int row = lane >> 3, pos = (lane >> 1) & 3; int tile = pos ^ ((row >> 1) & 3);
    wlo = (row + 16 * (w & 3)) * (F_ * 2) + tile * 32 + (lane & 1) * 16 + (G * 9 + 2 * j) * 128;
    w2lo = ((lane >> 1) + 32 * (w & 3)) * (F_ * 2) + (lane & 1) * 16 + (G * 9 + 8) * 128 + j * 32; }
  unsigned wdst = sb + gu * 8192 + (w & 3) * 2048;
  unsigned wdst2 = sb + gu * 4096 + (w & 3) * 1024;
  unsigned c2dst = sb + C2OFF + gu * 18432 + (w & 3) * 1024;
  u32x4 X[36];
  unsigned sq[5];
  const char* xb[2][3];
  for (int k = 0; k < 2; ++k) for (int i = 0; i < 3; ++i) { xb[k][i] = ws + WS_HC + (size_t)(tb + k) * 18432 + 4096 + i * 8192; opq_s(xb[k][i]); }
  char* ab[2];
  for (int k = 0; k < 2; ++k) { ab[k] = ws + WS_ACT + (size_t)(tb + k) * 144 * 1024 + (size_t)(G * 18 + 4 * j) * 1024; opq_s(ab[k]); }
  char* ab2 = ws + WS_ACT + (size_t)tb * 144 * 1024 + (size_t)(G * 18 + 16 + (j >> 1)) * 1024 + (j & 1) * 512; opq_s(ab2);
  const unsigned l16 = lane * 16;
  unsigned so;
  float sdv[2], nav[2];
  f32x4 acc[5][4];
  float gv[5][8];
  unsigned ov[5][4];
  auto vm_set = [&](auto Qc) AINL {
    constexpr int q = decltype(Qc)::v, iw = SC.opat(q, OW), ic = SC.opat(q, OC2);
    if constexpr (iw >= 0 && iw < 18) dma_set<((iw >> 1) * 64 + (iw & 1) * 8) * (F_ * 2), ((iw >> 1) % NSLOT) * 16384 + (iw & 1) * 1024>(so, wdst);
    if constexpr (iw >= 18) { constexpr int hs = iw - 18;
      dma_set<(hs % 9) * 64 * (F_ * 2) + (hs / 9) * 64, ((iw >> 1) % NSLOT) * 16384 + (iw & 1) * 8192>(so, wdst2); }
    if constexpr (ic >= 0) dma_set<ic * 128 * (F_ * 2), ic * 4096>(so, c2dst);
  };
  auto vm_go = [&](auto Qc) AINL {
    constexpr int q = decltype(Qc)::v, iw = SC.opat(q, OW), ic = SC.opat(q, OC2), ix = SC.opat(q, OX), is = SC.opat(q, OST);
    if constexpr (iw >= 0 && !(K3_SKIP & 1)) dma_go(rsW, wlo, so);
    if constexpr (ic >= 0 && !(K3_SKIP & 2)) dma_go(rsW, w2lo, so);
    if constexpr (ix >= 0 && !(K3_SKIP & 8)) gload16<((ix >> 1) & 7) * 1024 - 4096>(X[ix], xb[ix & 1][ix >> 4], l16);
    if constexpr (ix >= 0 && (K3_SKIP & 8)) asm volatile("" : "=v"(X[ix]));
    if constexpr (is == 4) {
      const unsigned off = ((lane >> 4) & 1) * (144 * 1024) + (lane >> 5) * 256 + (lane & 15) * 16;
      gstore16_act<0>(ab2, off, u32x4{ov[4][0], ov[4][1], ov[4][2], ov[4][3]});
    } else if constexpr (is >= 0) {
      constexpr int i2 = is > 4 ? is - 1 : is, pr = i2 >> 1, tk = i2 & 1, a = (pr & 1) * 2;
      gstore16_act<pr * 1024>(ab[tk], l16, u32x4{ov[a][2 * tk], ov[a][2 * tk + 1], ov[a + 1][2 * tk], ov[a + 1][2 * tk + 1]});
    }
  };
  auto vop = [&](auto opc, auto slc, auto vc) AINL {
    constexpr int op = decltype(opc)::v, sl = decltype(slc)::v, v = decltype(vc)::v, tk = v >> 2;
    if constexpr (op == VM1) a_mul(gv[sl][v], acc[sl][tk][v & 3], nav[tk]);
    else if constexpr (op == VEX) a_exp(gv[sl][v]);
    else if constexpr (op == VFM) a_fma11(gv[sl][v], sdv[tk]);
    else if constexpr (op == VRC) a_rcp(gv[sl][v]);
    else if constexpr (op == VM2) a_mul(gv[sl][v], gv[sl][v], acc[sl][2 + tk][v & 3]);
    else if constexpr (op == VCV) a_cvt(ov[sl][v], gv[sl][2 * v], gv[sl][2 * v + 1]);
    else a_pl16(ov[sl][v], ov[sl][2 + v]);
  };
  auto valu = [&](auto Qc) AINL {
    constexpr int q = decltype(Qc)::v;
    sfor<SC.vfirst(q), SC.vfirst(q + 1)>([&](auto jc) AINL { constexpr int jj = decltype(jc)::v; vop(IC<SC.vop[jj]>{}, IC<SC.vsl[jj]>{}, IC<SC.vix[jj]>{}); });
  };
  sfor<0, 17>([&](auto jc) AINL {
    constexpr int jj = decltype(jc)::v, kd = SC.kind[jj], id = SC.idx[jj];
    static_assert(SC.Q[jj] == -1 && SC.Q[17] >= 0, "prologue");
    if constexpr (kd == OW) { dma_set<((id >> 1) * 64 + (id & 1) * 8) * (F_ * 2), (id >> 1) * 16384 + (id & 1) * 1024>(so, wdst); dma_go(rsW, wlo, so); }
    else if constexpr (kd == OX) gload16<((id >> 1) & 7) * 1024 - 4096>(X[id], xb[id & 1][id >> 4], l16);
    else if constexpr (kd == OSQ) gload4<0>(sq[id], ws + WS_SSQ + (size_t)tb * 64 + id * 2 * 8192, (lane & 31) * 4 + (lane >> 5) * 8192);
  });

  unsigned ra[4];
  const int rrow = (lane >> 4) * 4 + ((lane & 15) >> 2);
  for (int t = 0; t < 4; ++t) ra[t] = sb + rrow * 128 + ((t ^ ((rrow >> 1) & 3)) * 32) + (lane & 3) * 8;
  u32x2 A[4][2];
  auto ldA = [&](auto uc, auto hc) AINL {
    constexpr int u = decltype(uc)::v, h = decltype(hc)::v;
    if constexpr (u < U1) {
      constexpr int t = u & 3, g = u >> 4, k = (u >> 2) & 3, slot = g % NSLOT;
      trd<(slot & 3) * 16384 + (k & 1) * 8192 + (k >> 1) * 4096 + h * 2048>(A[u & 3][h], ra[t]);
    } else if constexpr (u < U2) {
      constexpr int n = u - U1;
      trd<(n & 1) * 18432 + (n >> 1) * 1024 + h * 512>(A[u & 3][h], ra[0]);
    } else if constexpr (u < UE) {
      constexpr int n = u - U2, slot = (9 + (n >> 4)) % NSLOT;
      trd<(slot & 3) * 16384 + ((n >> 3) & 1) * 8192 + ((n >> 1) & 1) * 4096 + ((n >> 2) & 1) * 2048 + h * 1024>(A[u & 3][h], ra[2 + (n & 1)]);
    }
  };
  auto fixA = [&](auto uc) AINL {
    constexpr int u = decltype(uc)::v;
    if constexpr (u < U1 && u >= 16 && (u & 15) < 4) {
      constexpr int g = u >> 4, a = (g % NSLOT) >> 2, p = ((g - 1) % NSLOT) >> 2;
      if constexpr (a != p) vadd_opq<(a - p) * 65536>(ra[u & 3]);
    }
    if constexpr (u == U1) { ra[0] = sb + C2OFF + rrow * 32 + (lane & 3) * 8; opq_v(ra[0]); }
    if constexpr (u == U2 || u == U2 + 1) { ra[2 + u - U2] = sb + rrow * 64 + (((u - U2) ^ ((rrow >> 2) & 1)) * 32) + (lane & 3) * 8; opq_v(ra[2 + u - U2]); }
    if constexpr (u >= U2 + 16 && u < UE && ((u - U2) & 15) < 2) {
      constexpr int g = 9 + ((u - U2) >> 4), a = (g % NSLOT) >> 2, p = ((g - 1) % NSLOT) >> 2;
      if constexpr (a != p) vadd_opq<(a - p) * 65536>(ra[2 + (u & 1)]);
    }
  };
  wait_vm<SC.cnt(0, OW, 1)>();
  abar();
  STAMP_MID(1);
  sfor<0, 3>([&](auto uc) AINL { ldA(uc, IC<0>{}); ldA(uc, IC<1>{}); });
  sfor<0, UE>([&](auto uc) AINL {
    constexpr int u = decltype(uc)::v;
    if constexpr (u == U1) { STAMP_MID(2); }
    if constexpr (u == U2) { STAMP_MID(3); }
    if constexpr (u == U3) { STAMP_MID(4); }
    if constexpr (u == Sched::uc2(4)) {
      if ((w & 3) >= 2) { w2lo -= 64 * (F_ * 2); c2dst -= 2048; }
      opq_v(w2lo); opq_s(c2dst);
    }
    if constexpr (u == Sched::uw(9, 0)) {
      int row = lane >> 2, tile = ((lane >> 1) & 1) ^ ((row >> 2) & 1);
      wlo = (row + 16 * (w & 3)) * (F_ * 2) + tile * 32 + (lane & 1) * 16 + (G * 9 + 2 * j + 1) * 128; opq_v(wlo);
    }
    wait_vl<SC.vmat(u), (u >= UE - 1 ? 0 : u == UE - 2 ? 2 : 4)>();
    if constexpr (SC.barat(u) >= 0) abar();
    if constexpr (u == 16) {
      opq_v(sq[0]); opq_v(sq[1]); opq_v(sq[2]); opq_v(sq[3]); opq_v(sq[4]);
      float s = (__uint_as_float(sq[0]) + __uint_as_float(sq[1])) + (__uint_as_float(sq[2]) + __uint_as_float(sq[3])) + (lane < 32 ? __uint_as_float(sq[4]) : 0.f);
      auto sw = __builtin_amdgcn_permlane32_swap(__float_as_uint(s), __float_as_uint(s), false, false);
      s = __uint_as_float(sw[0]) + __uint_as_float(sw[1]);
      float var = rbf(s * (1.f / D_));
      float sd = rbf(__builtin_sqrtf(rbf(var + 1e-5f)));
      auto s2 = __builtin_amdgcn_permlane16_swap(__float_as_uint(sd), __float_as_uint(sd), false, false);
      sdv[0] = __uint_as_float(s2[0]); sdv[1] = __uint_as_float(s2[1]);
      nav[0] = -1.44269504088896f * __builtin_amdgcn_rcpf(sdv[0]); nav[1] = -1.44269504088896f * __builtin_amdgcn_rcpf(sdv[1]);
      opq_v(sdv[0]); opq_v(sdv[1]); opq_v(nav[0]); opq_v(nav[1]);
    }
    constexpr int sl = u < U1 ? (u & 3) : u < U2 ? 4 : u < U3 ? (u & 1) : 2 + (u & 1);
    constexpr int m2 = u < U1 ? ((u >> 2) & 1) * 2 : u < U2 ? (u & 1) * 2 : (u & 2);
    constexpr int ks = u < U1 ? u >> 3 : u < U2 ? (u - U1) >> 1 : u < U3 ? (u - U2) >> 2 : (u - U3) >> 2;
    const u32x4 av = cat4(A[u & 3][0], A[u & 3][1]);
    if constexpr (ks == 0) mf0(acc[sl][m2], av, X[0]); else mf(acc[sl][m2], av, X[2 * ks]);
    ldA(IC<u + 3>{}, IC<0>{});
    valu(IC<2 * u>{});
    vm_set(IC<2 * u + 1>{});
    vm_go(IC<2 * u>{});
    if constexpr (ks == 0) mf0(acc[sl][m2 + 1], av, X[1]); else mf(acc[sl][m2 + 1], av, X[2 * ks + 1]);
    ldA(IC<u + 3>{}, IC<1>{});
    valu(IC<2 * u + 1>{});
    vm_go(IC<2 * u + 1>{});
    fixA(IC<u + 4>{});
  });
  STAMP_MID(5);
  asm volatile("s_nop 7\n\ts_nop 7");
  sfor<VM1, VCV + 1>([&](auto opc) AINL {
    sfor<2, 4>([&](auto slc) AINL { sfor<0, (decltype(opc)::v == VCV ? 4 : 8)>([&](auto vc) AINL { vop(opc, slc, vc); }); });
  });
  sfor<0, 2>([&](auto tc) AINL { constexpr int tk = decltype(tc)::v;
    gstore16_act<3 * 1024>(ab[tk], l16, u32x4{ov[2][2 * tk], ov[2][2 * tk + 1], ov[3][2 * tk], ov[3][2 * tk + 1]}); });
  STAMP_MID(6);
  STAMP_END(ws, 2);
}

namespace k4 {
enum { OA = 0, ORW, ORR, OADD, OCVT, OWS, OW, OX, OH, OPS, OPL, NK };
constexpr int QO = 2 * LA + 8, NQ = 2 * UE + QO + 1;
struct Sched {
  int n; short Q[640], K[640], I[640];
  short first[NQ + 1];
  short loc[NK][170];
  short cvm[641], clg[641];
  signed char vm[2 * UE + 1], lg[2 * UE + 1], bar[UE + 1];
  int n0; short q0[640], k0[640], i0[640];
  int vm0;
  constexpr void add(int q, int k, int i) { q0[n0] = q; k0[n0] = k; i0[n0] = i; ++n0; }
  constexpr int cnt(int q, int k, int i) const {
    int j = loc[k][i], f = first[q + QO];
    return k >= OW ? cvm[f] - cvm[j + 1] : clg[f] - clg[j + 1];
  }
  constexpr void need(int q, int k, int i) {
    int c = cnt(q, k, i);
    if (Q[loc[k][i]] >= q) c = -100;
    if (k >= OW) { if (c < vm[q]) vm[q] = c; } else { if (c < lg[q]) lg[q] = c; }
  }
  constexpr Sched() : n(0), Q{}, K{}, I{}, first{}, loc{}, cvm{}, clg{}, vm{}, lg{}, bar{}, n0(0), q0{}, k0{}, i0{}, vm0(0) {
    int ub[40] = {}, nb = 0;
    int sbar[2] = {UA + 3, UB + 3};
    for (int u = 5; u < UA - 4; u += 8) ub[nb++] = u;
    ub[nb] = UA - 3 > ub[nb - 1] + 2 ? UA - 3 : 0; if (ub[nb]) ++nb;
    ub[nb++] = UA + 3;
    if (UA < UB) { ub[nb++] = UA + 11; ub[nb++] = UB + 3; }
    ub[nb++] = UB + 11; ub[nb++] = UB + 19;
    for (int j = 0; j < nb; ++j) bar[ub[j]] = 1;
    for (int u = 3; u < UE; ++u) add(2 * (u - 3), OA, u);
    int qadd[7] = {}, qps[4] = {};
    for (int g = 0; g < 2; ++g) {
      int a = g ? 6 : 0, b = g ? 7 : N0, E = g ? UB : UA;
      if (g && N0 == 7) break;
      for (int s = a; s < b; ++s) add(2 * (E - (b - s)) + 4, ORW, s);
      int q = 2 * sbar[g];
      for (int s = a; s < b; ++s) add(q + s - a, ORR, s);
      q += 4;
      for (int s = a; s < b; ++s) {
        qadd[s] = q;
        for (int i = 0; i < 4; ++i) add(q + (i >> 1), OADD, 4 * s + i);
        for (int i = 0; i < 2; ++i) add(q + 2, OCVT, 2 * s + i);
        q += 3;
        if ((s & 1) || s == 6) qps[s >> 1] = q;
      }
    }
    int pw = -1000;
    for (int r = 0; r < NR; ++r) {
      int u = 4 * r - LA;
      if (4 * r >= UA) u = UA - LA + (4 * r - UA) / K4_FAST;
      if (r >= RR_) { int j = 0; while (ub[j] < 4 * (r - RR_) + 3) ++j; if (u < ub[j]) u = ub[j]; }
      if (2 * u + 1 < pw + 2) u = (pw + 1) / 2;
      pw = 2 * u + 1;
      add(pw - 1, OWS, r); add(pw, OW, r);
    }
    for (int i = 0; i < 36; ++i) add(2 * (N0 * (i >> 1) - LA + 2 * (i & 1)) + 2, OX, i);
    for (int i = 0; i < 2; ++i) add(2 * (UPL - 8) + 2 * i, OH, i);
    for (int i = 0; i < 4; ++i) add(qps[i], OPS, i);
    for (int i = 0; i < 6; ++i) add(2 * UPL + 2 * i, OPL, i);
    short cn[NQ + 1] = {};
    for (int j = 0; j < n0; ++j) ++cn[q0[j] + QO];
    int acc = 0;
    for (int q = 0; q <= NQ; ++q) { first[q] = acc; acc += cn[q]; cn[q] = first[q]; }
    for (int j = 0; j < n0; ++j) { int p = cn[q0[j] + QO]++; Q[p] = q0[j]; K[p] = k0[j]; I[p] = i0[j]; loc[k0[j]][i0[j]] = p; }
    n = n0;
    for (int j = 0; j < n; ++j) { cvm[j + 1] = cvm[j] + (K[j] >= OW); clg[j + 1] = clg[j] + (K[j] <= ORR); }
    for (int q = 0; q <= 2 * UE; ++q) { vm[q] = 63; lg[q] = 15; }
    for (int u = 3; u < UE; ++u) need(2 * u, OA, u);
    for (int k = 1; k < 18; ++k) need(2 * N0 * k, OX, 2 * k + 1);
    for (int j = 0; j < nb; ++j) { int r = j + 1 < nb ? (ub[j + 1] + 2) / 4 : NR - 1; need(2 * ub[j], OW, r); }
    need(2 * sbar[0], ORW, N0 - 1);
    if (N0 < 7) need(2 * sbar[1], ORW, 6);
    for (int s = 0; s < 7; ++s) need(qadd[s], ORR, s);
    int c1 = cnt(0, OW, (ub[0] + 2) / 4), c2 = cnt(0, OX, 1);
    vm0 = c1 < c2 ? c1 : c2;
  }
  constexpr bool ok() const { for (int q = 0; q <= 2 * UE; ++q) if (vm[q] < 0 || lg[q] < 0) return false; return Q[n - 1] < 2 * UE; }
};
constexpr Sched SC{};
static_assert(SC.ok(), "schedule");
}

__global__ __launch_bounds__(512, 2) void k4_down(char* __restrict__ ws, u16* __restrict__ out) {
  using namespace k4;
  __shared__ __attribute__((aligned(1024))) char smem[RBO + 8 * RBS];
  STAMP_BEGIN();
  const unsigned sb = (unsigned)(size_t)smem;
  const int w = __builtin_amdgcn_readfirstlane(threadIdx.x >> 6);
  const int lane = threadIdx.x & 63;
  const int b = blockIdx.x, q = b & 3, bi = b >> 3;
  const int tt = ((b >> 2) & 1) * 8 + (bi >> 2), cg = bi & 3;
  const int m = w >> 1, kh = w & 1;
  const int tb = tt * 8 + w;
  const u32x4 rsW = mkrsrc(ws + WS_WDP + (size_t)(q * 4 + cg) * (324 * 1024), 324 * 1024);
  const unsigned l16 = lane * 16;
  const unsigned wlo = l16 + w * 1024;
  const unsigned wdst = sb + w * 1024;
  const char* xb[2][3];
  for (int k = 0; k < 2; ++k) for (int i = 0; i < 3; ++i) { xb[k][i] = ws + WS_ACT + ((size_t)(tb ^ k) * 144 + q * 36 + kh * 18) * 1024 + 4096 + i * 8192; opq_s(xb[k][i]); }
  unsigned ra[2] = {sb + kh * 1024 + l16, sb + kh * 1024 + l16 + 65536};
  unsigned rwa = sb + RBO + w * RBS + l16, rra = sb + RBO + (w ^ 1) * RBS + l16;
  opq_v(ra[0]); opq_v(ra[1]); opq_v(rwa); opq_v(rra);
  char* pb = ws + WS_PART + (size_t)(b & ~3) * PART_BLK;
  char* psb[4]; const char* plb[2]; const char* hb[2];
  for (int i = 0; i < 3; ++i) { psb[i] = pb + (size_t)((q + 1 + i) & 3) * PART_BLK + w * 3072 + i * 1024; opq_s(psb[i]); }
  psb[3] = pb + (size_t)m * PART_BLK + 24576 + ((m - q - 1) & 3) * 1024 + kh * 512; opq_s(psb[3]);
  plb[0] = pb + (size_t)q * PART_BLK + w * 3072; plb[1] = pb + (size_t)q * PART_BLK + 24576 + kh * 512; opq_s(plb[0]); opq_s(plb[1]);
  hb[0] = ws + WS_HF + ((size_t)tb * 18 + (cg >> 1) * 9 + (cg & 1) * 5 + q) * 1024;
  hb[1] = ws + WS_HF + ((size_t)tb * 18 + (cg >> 1) * 9 + 4) * 1024 + (cg & 1) * 8; opq_s(hb[0]); opq_s(hb[1]);
  const int tj = tb_j(tb);
  char* ob = (char*)out + (size_t)tb_first(tb, tj) * 1152 + (cg >> 1) * 576;
  char* ob0 = ob + ((cg & 1) * 5 + q) * 64; char* ob1 = ob + 256 + (cg & 1) * 32; opq_s(ob0); opq_s(ob1);
  unsigned l8 = lane * 8, l8m = m == q ? lane * 8 : 0; opq_v(l8); opq_v(l8m);
  u32x4 X[36], A[4], P[4], sel0, sel1, selb;
  u32x2 Ps[4];
  f32x4 acc[9][2], rr[7];
  unsigned ov[7][2], oo0, oo1;
  unsigned so;
  auto op = [&](auto jc) AINL {
    constexpr int j = decltype(jc)::v, k = SC.K[j], i = SC.I[j];
    if constexpr (k == OA) { constexpr int o = (2 * i % RING) * 1024; rd128<(o & 65535)>(A[i & 3], ra[o >> 16]); }
    else if constexpr (k == ORW) wr128<i % N0 * 1024>(rwa, acc[i][1]);
    else if constexpr (k == ORR) rd128f<i % N0 * 1024>(rr[i], rra);
    else if constexpr (k == OADD) { float t = acc[i >> 2][0][i & 3]; a_add(t, rr[i >> 2][i & 3]); acc[i >> 2][0][i & 3] = t; }
    else if constexpr (k == OCVT) a_cvt(ov[i >> 1][i & 1], acc[i >> 1][0][2 * (i & 1)], acc[i >> 1][0][2 * (i & 1) + 1]);
    else if constexpr (k == OWS) dma_set<i * 8192, (8 * i % RING) * 1024>(so, wdst);
    else if constexpr (k == OW) dma_go(rsW, wlo, so);
    else if constexpr (k == OX) gload16<((i >> 1) & 7) * 1024 - 4096>(X[i], xb[i & 1][i >> 4], l16);
    else if constexpr (k == OH) { if constexpr (i == 0) gload16<0>(P[3], hb[0], l16); else gload8<0>(Ps[3], hb[1], l16); }
    else if constexpr (k == OPS) {
      if constexpr (i < 3) gstore16_wt<0>(psb[i], l16, u32x4{ov[2 * i][0], ov[2 * i][1], ov[2 * i + 1][0], ov[2 * i + 1][1]});
      else gstore8_wt<0>(psb[3], l8, u32x2{ov[6][0], ov[6][1]});
    } else if constexpr (k == OPL) {
      if constexpr (i < 3) gload16_c<i * 1024>(P[i], plb[0], l16); else gload8_c<(i - 3) * 1024>(Ps[i - 3], plb[1], l8m);
    }
  };
  sfor<0, SC.first[QO]>(op);
  wait_vm<SC.vm0>();
  abar();
  STAMP_MID(1);
  sfor<0, 3>([&](auto uc) AINL { constexpr int u = decltype(uc)::v; rd128<2048 * u>(A[u], ra[0]); });
  sfor<0, 2 * UE>([&](auto qc) AINL {
    constexpr int g = decltype(qc)::v, u = g >> 1, i = g & 1, sl = slot_of(u), ks = ks_of(u);
    if constexpr (g == 2 * UA) { STAMP_MID(2); }
    if constexpr (g == 2 * UB + 16) { STAMP_MID(3); }
    wait_vl2<SC.vm[g], (g < 6 && i == 0 ? 2 : SC.lg[g])>();
    if constexpr (i == 0 && SC.bar[u]) abar();
    if constexpr (ks == 0) mf0(acc[sl][i], A[u & 3], X[i]); else mf(acc[sl][i], A[u & 3], X[2 * ks + i]);
    sfor<SC.first[g + QO], SC.first[g + 1 + QO]>(op);
    if constexpr (g == 2 * UE - 21) {
      opq_v(l8);
      const unsigned one = ((l8 >> 5) & 3) == (l8 >> 7) ? 0x3f80u << ((l8 & 8) * 2) : 0u;
      const unsigned d0 = (l8 & 16) ? 0u : one, d1 = (l8 & 16) ? one : 0u;
      selb = u32x4{d0, d1, d0, d1}; opq_v(selb);
    }
    if constexpr (g == 2 * UE - 17) { sel0 = u32x4{selb[0], selb[1], 0u, 0u}; opq_v(sel0); }
    if constexpr (g == 2 * UE - 15) { sel1 = u32x4{0u, 0u, selb[0], selb[1]}; opq_v(sel1); }
    if constexpr (g == 2 * UE - 13) { opq_v(l8); oo1 = tb_tok(tj, (l8 >> 3) & 15) * 1152; oo0 = oo1 + ((l8 >> 7) & 1) * 32 + (l8 >> 8) * 16; oo1 += (l8 >> 7) * 8; opq_v(oo0); opq_v(oo1); }
  });
  STAMP_MID(4);
  asm volatile("s_nop 7\n\ts_nop 3");
  wr128<1024>(rwa, acc[7][1]); wr128<2048>(rwa, acc[8][1]);
  wait_vl2<63, 0>();
  abar();
  rd128f<1024>(rr[0], rra); rd128f<2048>(rr[1], rra);
  asm volatile("s_waitcnt vmcnt(0) lgkmcnt(0)" : "+v"(P[0]), "+v"(P[1]), "+v"(P[2]), "+v"(P[3]), "+v"(Ps[0]), "+v"(Ps[1]), "+v"(Ps[2]), "+v"(Ps[3]), "+v"(rr[0]), "+v"(rr[1])::"memory");
  auto missing = [&]() AINL {
    unsigned mx = umax3(umax3(P[0][0], P[0][1], P[0][2]), umax3(P[0][3], P[1][0], P[1][1]), umax3(P[1][2], P[1][3], P[2][0]));
    mx = umax3(mx, P[2][1], __builtin_elementwise_max(P[2][2], P[2][3]));
    if (m == q) mx = umax3(mx, umax3(Ps[0][0], Ps[0][1], Ps[1][0]), umax3(Ps[1][1], Ps[2][0], Ps[2][1]));
    return __builtin_amdgcn_ballot_w64(mx == 0xffffffffu) != 0;
  };
#pragma unroll 1
  for (int spin = 0; __builtin_expect(missing(), 0) && spin < (1 << 16); ++spin) {
    asm volatile("s_sleep 1\n\tglobal_load_dwordx4 %0, %6, %8 sc1\n\tglobal_load_dwordx4 %1, %6, %8 offset:1024 sc1\n\tglobal_load_dwordx4 %2, %6, %8 offset:2048 sc1\n\t"
                 "global_load_dwordx2 %3, %7, %9 sc1\n\tglobal_load_dwordx2 %4, %7, %9 offset:1024 sc1\n\tglobal_load_dwordx2 %5, %7, %9 offset:2048 sc1\n\ts_waitcnt vmcnt(0)"
                 : "+v"(P[0]), "+v"(P[1]), "+v"(P[2]), "+v"(Ps[0]), "+v"(Ps[1]), "+v"(Ps[2]) : "v"(l16), "v"(l8m), "s"(plb[0]), "s"(plb[1]) : "memory");
  }
  asm volatile("s_nop 1" : "+v"(P[0]), "+v"(P[1]), "+v"(P[2]), "+v"(Ps[0]), "+v"(Ps[1]), "+v"(Ps[2]), "+v"(Ps[3]));
  STAMP_MID(5);
  sfor<0, 4>([&](auto ic) AINL { constexpr int i = decltype(ic)::v; mf(acc[7][0], sel0, P[i]); mf(acc[8][0], sel1, P[i]); });
  if (m == q) { mf(acc[6][0], selb, cat4(Ps[0], Ps[1])); mf(acc[6][0], selb, cat4(Ps[2], Ps[3])); }
  asm volatile("s_nop 7\n\ts_nop 3" : "+v"(acc[6][0]), "+v"(acc[7][0]), "+v"(acc[8][0])::"memory");
  { const f32x4 a = acc[7][0] + rr[0], c = acc[8][0] + rr[1];
    auto s0 = __builtin_amdgcn_permlane16_swap(pk2(a[0], a[1]), pk2(c[0], c[1]), false, false);
    auto s1 = __builtin_amdgcn_permlane16_swap(pk2(a[2], a[3]), pk2(c[2], c[3]), false, false);
    gstore16_wt<0>(ob0, oo0, u32x4{s0[0], s1[0], s0[1], s1[1]});
    if (m == q) gstore8_wt<0>(ob1, oo1, u32x2{pk2(acc[6][0][0], acc[6][0][1]), pk2(acc[6][0][2], acc[6][0][3])});
  }
  STAMP_END(ws, 3);
  asm volatile("k_text1:");
}

static constexpr int g_mask = 15;
extern "C" void kernel_launch(void* const* d_in, const int* in_sizes, int n_in, void* d_out, int out_size, void* d_ws, size_t ws_size,
                              hipStream_t stream) {
  const u16* x = (const u16*)d_in[0];
  const u16* wqkv = (const u16*)d_in[1];
  const u16* wo = (const u16*)d_in[2];
  const u16* wg = (const u16*)d_in[4];
  const u16* wu = (const u16*)d_in[5];
  const u16* wd = (const u16*)d_in[6];
  char* ws = (char*)d_ws;
  if (g_mask & 1) k1_attn<<<256, 768, 0, stream>>>(x, wqkv, wo, wg, wu, wd, ws);
  if (g_mask & 4) k3_gateup<<<256, 512, 0, stream>>>(wg, wu, ws);
  if (g_mask & 8) k4_down<<<256, 512, 0, stream>>>(ws, (u16*)d_out);
}
```

```cpp
#include <hip/hip_runtime.h>
#include <stdint.h>
typedef unsigned short u16;
typedef unsigned long long u64;
__device__ __forceinline__ float bf2f(u16 v) { return __uint_as_float(((unsigned)v) << 16); }
__device__ __forceinline__ u16 f2bf(float f) { __bf16 b = (__bf16)f; return __builtin_bit_cast(u16, b); }
__device__ __forceinline__ float rbf(float f) { return (float)(__bf16)f; }
constexpr int D = 576, F = 4608;
__global__ __launch_bounds__(512) void naive(const u16* x, const u16* wqkv, const u16* wo, const u16* wg, const u16* wu, const u16* wd, u16* out) {
  u64 t0 = __builtin_amdgcn_s_memrealtime();
  __shared__ float xn[22][D];
  __shared__ u16 big[22 * 1728];
  __shared__ float h[8][D];
  const int tid = threadIdx.x, tb = blockIdx.x * 8, w = tid >> 6, lane = tid & 63;
  for (int r = w; r < 22; r += 8) {
    int t = max(tb - 14 + r, 0);
    float s = 0; for (int i = lane; i < D; i += 64) s += bf2f(x[t * D + i]);
    for (int o = 32; o; o >>= 1) s += __shfl_xor(s, o);
    float m = s / D, v = 0;
    for (int i = lane; i < D; i += 64) { float d = bf2f(x[t * D + i]) - m; v += d * d; }
    for (int o = 32; o; o >>= 1) v += __shfl_xor(v, o);
    float rs = rsqrtf(v / D + 1e-5f);
    for (int i = lane; i < D; i += 64) xn[r][i] = rbf((bf2f(x[t * D + i]) - m) * rs);
  }
  __syncthreads();
  for (int n = tid; n < 1728; n += 512) {
    float a[22] = {};
    for (int k = 0; k < D; ++k) { float wv = bf2f(wqkv[k * 1728 + n]);
#pragma unroll
      for (int r = 0; r < 22; ++r) a[r] += xn[r][k] * wv; }
#pragma unroll
    for (int r = 0; r < 22; ++r) big[r * 1728 + n] = f2bf(a[r]);
  }
  __syncthreads();
  if (tid < 256) {
    int r = tid >> 5, hd = tid & 31, t = tb + r;
    float lg[8], mx = -1e30f, sum = 0;
#pragma unroll
    for (int j = 0; j < 8; ++j) { float a = 0;
      for (int d = 0; d < 18; ++d) a += bf2f(big[(14 + r) * 1728 + hd * 18 + d]) * bf2f(big[(14 + r - 2 * j) * 1728 + D + hd * 18 + d]);
      lg[j] = t - 2 * j < 0 ? -1e9f : a * 0.23570226f; mx = fmaxf(mx, lg[j]); }
#pragma unroll
    for (int j = 0; j < 8; ++j) { lg[j] = expf(lg[j] - mx); sum += lg[j]; }
    for (int d = 0; d < 18; ++d) { float a = 0;
#pragma unroll
      for (int j = 0; j < 8; ++j) a += lg[j] / sum * bf2f(big[(14 + r - 2 * j) * 1728 + 2 * D + hd * 18 + d]);
      xn[r][hd * 18 + d] = rbf(a); }
  }
  __syncthreads();
  for (int n = tid; n < D; n += 512) {
    float a[8] = {};
    for (int k = 0; k < D; ++k) { float wv = bf2f(wo[k * D + n]);
#pragma unroll
      for (int r = 0; r < 8; ++r) a[r] += xn[r][k] * wv; }
#pragma unroll
    for (int r = 0; r < 8; ++r) h[r][n] = rbf(bf2f(x[(tb + r) * D + n]) + rbf(a[r]));
  }
  __syncthreads();
  { int r = w;
    float s = 0; for (int i = lane; i < D; i += 64) s += h[r][i];
    for (int o = 32; o; o >>= 1) s += __shfl_xor(s, o);
    float m = s / D, v = 0;
    for (int i = lane; i < D; i += 64) { float d = h[r][i] - m; v += d * d; }
    for (int o = 32; o; o >>= 1) v += __shfl_xor(v, o);
    float rs = rsqrtf(v / D + 1e-5f);
    for (int i = lane; i < D; i += 64) xn[r][i] = rbf((h[r][i] - m) * rs);
  }
  __syncthreads();
  for (int n = tid; n < F; n += 512) {
    float a[8] = {}, b[8] = {};
    for (int k = 0; k < D; ++k) { float g = bf2f(wg[k * F + n]), u = bf2f(wu[k * F + n]);
#pragma unroll
      for (int r = 0; r < 8; ++r) { a[r] += xn[r][k] * g; b[r] += xn[r][k] * u; } }
#pragma unroll
    for (int r = 0; r < 8; ++r) big[r * F + n] = f2bf(b[r] / (1.f + expf(-a[r])));
  }
  __syncthreads();
  for (int n = tid; n < D; n += 512) {
    float a[8] = {};
    for (int k = 0; k < F; ++k) { float wv = bf2f(wd[k * D + n]);
#pragma unroll
      for (int r = 0; r < 8; ++r) a[r] += bf2f(big[r * F + k]) * wv; }
#pragma unroll
    for (int r = 0; r < 8; ++r) out[(tb + r) * D + n] = f2bf(h[r][n] + rbf(a[r]));
  }
  for (int i = 0; i < 10000000 && __builtin_amdgcn_s_memrealtime() - t0 < 0; ++i) __builtin_amdgcn_s_sleep(1);
}
__global__ void k_nop(char* ws) { if (ws == nullptr) ws[threadIdx.x] = 0; }
extern "C" void kernel_launch(void* const* d_in, const int* in_sizes, int n_in, void* d_out, int out_size, void* d_ws, size_t ws_size, hipStream_t stream) {
  naive<<<256, 512, 0, stream>>>((const u16*)d_in[0], (const u16*)d_in[1], (const u16*)d_in[2], (const u16*)d_in[4], (const u16*)d_in[5], (const u16*)d_in[6], (u16*)d_out);

}
```

```cpp
#include <hip/hip_runtime.h>
#include <stdint.h>

typedef unsigned short u16;
typedef unsigned long long u64;
typedef __attribute__((ext_vector_type(8))) __bf16 bf16x8;
typedef __attribute__((ext_vector_type(2))) __bf16 bf16x2;
typedef __attribute__((ext_vector_type(4))) short s16x4;
typedef __attribute__((ext_vector_type(4))) float f32x4;
typedef __attribute__((ext_vector_type(2))) float f32x2;
typedef __attribute__((ext_vector_type(16))) float f32x16;
typedef __attribute__((ext_vector_type(4))) unsigned u32x4;
typedef __attribute__((ext_vector_type(2))) unsigned u32x2;
typedef __attribute__((ext_vector_type(8))) unsigned u32x8;
#define DI __device__ __forceinline__
#define LDSP(T, a) ((__attribute__((address_space(3))) T*)(a))
#define AINL __attribute__((always_inline))

template <int I> struct IC { static constexpr int v = I; };
template <int B, int E, class F> DI void sfor(F&& f) {
  if constexpr (B + 1 == E) f(IC<B>{});
  else if constexpr (B < E) { sfor<B, (B + E) / 2>(f); sfor<(B + E) / 2, E>(f); }
}

constexpr int S_ = 2048, D_ = 576, F_ = 4608;

constexpr size_t WS_OF   = 0;
constexpr int OLB = 2176;
constexpr size_t WS_OL   = WS_OF + 128 * 16384;
constexpr size_t WS_HC   = WS_OL + 128 * OLB;
constexpr size_t WS_HF   = WS_HC + (size_t)S_ * D_ * 2;
constexpr size_t WS_ACT  = WS_HF + (size_t)S_ * D_ * 2;
constexpr size_t WS_PART = WS_ACT + (size_t)S_ * F_ * 2;
constexpr size_t PART_BLK = 28672;
constexpr size_t WS_WDP  = WS_PART + (size_t)256 * PART_BLK;
constexpr size_t WS_SSQ  = WS_WDP + (size_t)16 * 324 * 1024;
constexpr size_t WS_RM   = WS_SSQ + (size_t)10 * S_ * 4;
constexpr size_t WS_FR   = WS_RM + 4096;
constexpr size_t WS_FG   = WS_FR + 64 * 2048;
constexpr size_t WS_STAMP = WS_FG + 32 * 1024;

#define STAMP_BEGIN()
#define STAMP_MID(i)
#define STAMP_END(ws, k)
#define STAMP_ARG
#define STAMP_PASS

DI float bf2f(u16 v) { return __uint_as_float(((unsigned)v) << 16); }
DI float rbf(float f) { return (float)(__bf16)f; }
DI unsigned pk2(float a, float b) { bf16x2 v = {(__bf16)a, (__bf16)b}; return __builtin_bit_cast(unsigned, v); }
DI float lo16(unsigned u) { return __uint_as_float(u << 16); }
DI float hi16(unsigned u) { return __uint_as_float(u & 0xffff0000u); }

template <int N> DI void wait_vm() { asm volatile("s_waitcnt vmcnt(%0)" ::"i"(N) : "memory"); }
template <int N> DI void wait_vm_tie(u32x4& a) { asm volatile("s_waitcnt vmcnt(%1)" : "+v"(a) : "i"(N) : "memory"); }
DI void barrier() { __builtin_amdgcn_s_barrier(); }
#define SCHED() __builtin_amdgcn_sched_barrier(0)

DI void glds16(const void* sbase, unsigned voff, unsigned lds) {
  asm volatile("s_mov_b32 m0, %2\n\ts_nop 0\n\tglobal_load_lds_dwordx4 %0, %1" ::"v"(voff), "s"(sbase), "s"(lds) : "memory");
}
template <int C> DI void vadd_opq(unsigned& a) { asm volatile("v_add_u32 %0, %1, %0" : "+v"(a) : "i"(C)); }
template <int OFF> DI void gload16(u32x4& d, const void* sbase, unsigned voff) {
  asm volatile("global_load_dwordx4 %0, %1, %2 offset:%3" : "=v"(d) : "v"(voff), "s"(sbase), "i"(OFF) : "memory");
}
template <int OFF> DI void gload4(unsigned& d, const void* sbase, unsigned voff) {
  asm volatile("global_load_dword %0, %1, %2 offset:%3" : "=v"(d) : "v"(voff), "s"(sbase), "i"(OFF) : "memory");
}
template <int OFF> DI void gstore16_wt(void* sbase, unsigned voff, u32x4 v) {
  asm volatile("global_store_dwordx4 %0, %1, %2 offset:%3 sc1\n\ts_nop 1" ::"v"(voff), "v"(v), "s"(sbase), "i"(OFF) : "memory");
}
template <int LO> DI void glds_nt(const void* sbase, unsigned voff, unsigned lds) {
  asm volatile("s_add_u32 m0, %2, %3\n\ts_nop 0\n\tglobal_load_lds_dwordx4 %0, %1 nt" ::"v"(voff), "s"(sbase), "s"(lds), "i"(LO) : "memory", "scc");
}
template <class T> DI void undef(T& v) { asm volatile("" : "=v"(v)); }
#define ACT_POL "sc1"
template <int OFF> DI void gstore16_act(void* sbase, unsigned voff, u32x4 v) {
  asm volatile("global_store_dwordx4 %0, %1, %2 offset:%3 " ACT_POL "\n\ts_nop 1" ::"v"(voff), "v"(v), "s"(sbase), "i"(OFF) : "memory");
}
#define HC_POL "sc1"
#define OUT_POL "sc1"
template <int OFF> DI void gstore16_hc(void* sbase, unsigned voff, u32x4 v) {
  asm volatile("global_store_dwordx4 %0, %1, %2 offset:%3 " HC_POL "\n\ts_nop 1" ::"v"(voff), "v"(v), "s"(sbase), "i"(OFF) : "memory");
}
DI void gstore16_out(void* sbase, unsigned voff, u32x4 v) { asm volatile("global_store_dwordx4 %0, %1, %2 " OUT_POL "\n\ts_nop 1" ::"v"(voff), "v"(v), "s"(sbase) : "memory"); }
DI void gstore8_out(void* sbase, unsigned voff, u32x2 v) { asm volatile("global_store_dwordx2 %0, %1, %2 " OUT_POL "\n\ts_nop 1" ::"v"(voff), "v"(v), "s"(sbase) : "memory"); }
DI s16x4 trread(unsigned a) { return __builtin_amdgcn_ds_read_tr16_b64_v4i16(LDSP(s16x4, a)); }
DI bf16x8 cat8(s16x4 a, s16x4 b) { return __builtin_bit_cast(bf16x8, __builtin_shufflevector(a, b, 0, 1, 2, 3, 4, 5, 6, 7)); }
DI f32x16 mfma32(bf16x8 a, u32x4 b, f32x16 c) { return __builtin_amdgcn_mfma_f32_32x32x16_bf16(a, __builtin_bit_cast(bf16x8, b), c, 0, 0, 0); }
DI f32x4 mfma16(bf16x8 a, u32x4 b, f32x4 c) { return __builtin_amdgcn_mfma_f32_16x16x32_bf16(a, __builtin_bit_cast(bf16x8, b), c, 0, 0, 0); }

DI void mf(f32x4& c, const u32x4& a, const u32x4& b) { asm volatile("v_mfma_f32_16x16x32_bf16 %0, %1, %2, %0" : "+v"(c) : "v"(a), "v"(b)); }
DI void mf0(f32x4& c, const u32x4& a, const u32x4& b) { asm volatile("v_mfma_f32_16x16x32_bf16 %0, %1, %2, 0" : "=v"(c) : "v"(a), "v"(b)); }
template <int OFF> DI void trd(u32x2& d, unsigned a) { asm volatile("ds_read_b64_tr_b16 %0, %1 offset:%2" : "=v"(d) : "v"(a), "i"(OFF)); }
template <int OFF> DI void rd128(u32x4& d, unsigned a) { asm volatile("ds_read_b128 %0, %1 offset:%2" : "=v"(d) : "v"(a), "i"(OFF)); }
template <int V, int L> DI void wait_vl() {
  if constexpr (V >= 63) asm volatile("s_waitcnt lgkmcnt(%0)" ::"i"(L) : "memory");
  else asm volatile("s_waitcnt vmcnt(%0) lgkmcnt(%1)" ::"i"(V), "i"(L) : "memory");
}
DI void abar() { asm volatile("s_barrier" ::: "memory"); }
DI u32x4 mkrsrc(const void* p, unsigned bytes) { u32x4 r = {(unsigned)(size_t)p, (unsigned)((size_t)p >> 32) & 0xffffu, bytes, 0x20000u}; return r; }
template <int SO, int LO> DI void dma_set(unsigned& so, unsigned ldsbase) {
  asm volatile("s_add_u32 m0, %1, %3\n\ts_mov_b32 %0, %2" : "=s"(so) : "s"(ldsbase), "i"(SO), "i"(LO) : "scc");
}
DI void dma_setr(unsigned lds) { asm volatile("s_mov_b32 m0, %0" ::"s"(lds)); }
DI void dma_go(const u32x4& rs, unsigned voff, unsigned so) {
  asm volatile("buffer_load_dwordx4 %0, %1, %2 offen lds" ::"v"(voff), "s"(rs), "s"(so) : "memory");
}
template <class T> DI void opq_s(T& v) { asm volatile("" : "+s"(v)); }
template <class T> DI void opq_v(T& v) { asm volatile("" : "+v"(v)); }
DI u32x4 cat4(const u32x2& a, const u32x2& b) { return u32x4{a[0], a[1], b[0], b[1]}; }

DI void a_mul(float& d, float a, float b) { asm volatile("v_mul_f32 %0, %1, %2" : "=v"(d) : "v"(a), "v"(b)); }
DI void a_mul1(float& d, float b) { asm volatile("v_mul_f32 %0, %0, %1" : "+v"(d) : "v"(b)); }
DI void a_dot(float& d, unsigned a, unsigned b) { asm volatile("v_dot2c_f32_bf16 %0, %1, %2" : "+v"(d) : "v"(a), "v"(b)); }
DI void a_exp(float& d) { asm volatile("v_exp_f32 %0, %0" : "+v"(d)); }
DI void a_rcp(float& d) { asm volatile("v_rcp_f32 %0, %0" : "+v"(d)); }
DI void a_fma11(float& d, float s) { asm volatile("v_fma_f32 %0, %0, %1, %1" : "+v"(d) : "v"(s)); }
DI void a_cvt(unsigned& d, float a, float b) { asm volatile("v_cvt_pk_bf16_f32 %0, %1, %2" : "=v"(d) : "v"(a), "v"(b)); }
DI void a_pl16(unsigned& a, unsigned& b) { asm volatile("v_permlane16_swap_b32 %0, %1" : "+v"(a), "+v"(b)); }

template <int OFF> DI void wr128(unsigned a, const f32x4& v) { asm volatile("ds_write_b128 %0, %1 offset:%2" ::"v"(a), "v"(v), "i"(OFF) : "memory"); }
template <int OFF> DI void rd128f(f32x4& d, unsigned a) { asm volatile("ds_read_b128 %0, %1 offset:%2" : "=v"(d) : "v"(a), "i"(OFF)); }
DI void a_add(float& d, float a) { asm volatile("v_add_f32 %0, %0, %1" : "+v"(d) : "v"(a)); }
template <int OFF> DI void gload16_c(u32x4& d, const void* sbase, unsigned voff) {
  asm volatile("global_load_dwordx4 %0, %1, %2 offset:%3 sc1" : "=v"(d) : "v"(voff), "s"(sbase), "i"(OFF) : "memory");
}
template <int OFF> DI void gload8_c(u32x2& d, const void* sbase, unsigned voff) {
  asm volatile("global_load_dwordx2 %0, %1, %2 offset:%3 sc1" : "=v"(d) : "v"(voff), "s"(sbase), "i"(OFF) : "memory");
}
template <int OFF> DI void gload4_c(unsigned& d, const void* sbase, unsigned voff) {
  asm volatile("global_load_dword %0, %1, %2 offset:%3 sc1" : "=v"(d) : "v"(voff), "s"(sbase), "i"(OFF) : "memory");
}
template <int OFF> DI void gload8(u32x2& d, const void* sbase, unsigned voff) {
  asm volatile("global_load_dwordx2 %0, %1, %2 offset:%3" : "=v"(d) : "v"(voff), "s"(sbase), "i"(OFF) : "memory");
}
template <int OFF> DI void gstore8_wt(void* sbase, unsigned voff, u32x2 v) {
  asm volatile("global_store_dwordx2 %0, %1, %2 offset:%3 sc1\n\ts_nop 1" ::"v"(voff), "v"(v), "s"(sbase), "i"(OFF) : "memory");
}
template <int V, int L> DI void wait_vl2() {
  if constexpr (V >= 63 && L >= 15) {}
  else if constexpr (V >= 63) asm volatile("s_waitcnt lgkmcnt(%0)" ::"i"(L) : "memory");
  else if constexpr (L >= 15) asm volatile("s_waitcnt vmcnt(%0)" ::"i"(V) : "memory");
  else asm volatile("s_waitcnt vmcnt(%0) lgkmcnt(%1)" ::"i"(V), "i"(L) : "memory");
}
DI unsigned umax3(unsigned a, unsigned b, unsigned c) { return __builtin_elementwise_max(__builtin_elementwise_max(a, b), c); }

DI int tb_j(int tb) { return tb >= 126 ? tb - 121 : tb % 7; }
DI int tb_first(int tb, int j) { return 16 * tb - (j && !(j & 1) ? 15 : 0); }
DI int tb_tok(int j, int l) { return j ? 2 * l : 2 * (l & 7) + (l >> 3); }
DI int tb_row(int j, int l) { return j ? (~j & 1) * 64 + ((j + 1) >> 1) * 16 + l : (l >> 3) * 64 + 8 + (l & 7); }

DI float dot2(unsigned a, unsigned b, float c) { return __builtin_amdgcn_fdot2_f32_bf16(__builtin_bit_cast(bf16x2, a), __builtin_bit_cast(bf16x2, b), c, false); }
template <int C> DI float dpp_add(float v) { return v + __builtin_bit_cast(float, __builtin_amdgcn_update_dpp(0, __builtin_bit_cast(int, v), C, 0xf, 0xf, false)); }
DI float sum8(float v) { return dpp_add<0x141>(dpp_add<0x4e>(dpp_add<0xb1>(v))); }
DI float bc0(float v) { return __builtin_bit_cast(float, __builtin_amdgcn_update_dpp(0, __builtin_bit_cast(int, v), 0x150, 0xf, 0xf, true)); }
DI float xmax4(float v) {
  auto r = __builtin_amdgcn_permlane32_swap(__float_as_uint(v), __float_as_uint(v), false, false); v = __builtin_fmaxf(__uint_as_float(r[0]), __uint_as_float(r[1]));
  auto q = __builtin_amdgcn_permlane16_swap(__float_as_uint(v), __float_as_uint(v), false, false); return __builtin_fmaxf(__uint_as_float(q[0]), __uint_as_float(q[1]));
}
DI f32x4 mfb(const u32x4& a, const u32x4& b, const f32x4& c) { return __builtin_amdgcn_mfma_f32_16x16x32_bf16(__builtin_bit_cast(bf16x8, a), __builtin_bit_cast(bf16x8, b), c, 0, 0, 0); }
template <int OFF> DI void wr128u(unsigned a, const u32x4& v) { asm volatile("ds_write_b128 %0, %1 offset:%2" ::"v"(a), "v"(v), "i"(OFF) : "memory"); }
template <int OFF> DI void wr64u(unsigned a, const u32x2& v) { asm volatile("ds_write_b64 %0, %1 offset:%2" ::"v"(a), "v"(v), "i"(OFF) : "memory"); }
template <int OFF> DI void rd64f(f32x2& d, unsigned a) { asm volatile("ds_read_b64 %0, %1 offset:%2" : "=v"(d) : "v"(a), "i"(OFF)); }
template <int O0, int O1> DI void rd2f(f32x2& d, unsigned a) { asm volatile("ds_read2_b32 %0, %1 offset0:%2 offset1:%3" : "=v"(d) : "v"(a), "i"(O0), "i"(O1)); }
template <int OFF> DI void wr32u(unsigned a, unsigned v) { asm volatile("ds_write_b32 %0, %1 offset:%2" ::"v"(a), "v"(v), "i"(OFF) : "memory"); }
template <int OFF> DI void wr64f(unsigned a, const f32x2& v) { asm volatile("ds_write_b64 %0, %1 offset:%2" ::"v"(a), "v"(v), "i"(OFF) : "memory"); }
DI void bstore16(const u32x4& rs, unsigned vo, const u32x4& v) { asm volatile("buffer_store_dwordx4 %0, %1, %2, 0 offen sc1\n\ts_nop 1" ::"v"(v), "v"(vo), "s"(rs) : "memory"); }
DI void bstore4(const u32x4& rs, unsigned vo, unsigned v) { asm volatile("buffer_store_dword %0, %1, %2, 0 offen sc1" ::"v"(v), "v"(vo), "s"(rs) : "memory"); }
DI void gstore8_l8(void* sbase, unsigned voff, u32x2 v) { asm volatile("s_mov_b64 exec, 255\n\tglobal_store_dwordx2 %0, %1, %2 sc1\n\ts_mov_b64 exec, -1" ::"v"(voff), "v"(v), "s"(sbase) : "memory"); }
DI void gload8_d(u32x2& d, const void* sbase, unsigned voff) { asm volatile("global_load_dwordx2 %0, %1, %2 sc1" : "+v"(d) : "v"(voff), "s"(sbase) : "memory"); }
DI void gload16_d(const u32x4& d, const void* sbase, unsigned voff) { asm volatile("global_load_dwordx4 %0, %1, %2 sc1" ::"v"(d), "v"(voff), "s"(sbase) : "memory"); }
template <int OFF> DI void gload4_d(unsigned d, const void* sbase, unsigned voff) { asm volatile("global_load_dword %0, %1, %2 offset:%3 sc1" ::"v"(d), "v"(voff), "s"(sbase), "i"(OFF) : "memory"); }
DI void rd32(unsigned& d, unsigned a) { asm volatile("ds_read_b32 %0, %1" : "=v"(d) : "v"(a)); }
DI f32x2 sp2(float v) { return f32x2{v, v}; }
DI f32x2 lo2(const f32x4& v) { return f32x2{v[0], v[1]}; }
DI f32x2 hi2(const f32x4& v) { return f32x2{v[2], v[3]}; }
DI f32x2 h2(const f32x4& v, int e) { return e ? hi2(v) : lo2(v); }
DI f32x2 fma2(f32x2 a, f32x2 b, f32x2 c) { return __builtin_elementwise_fma(a, b, c); }
DI unsigned pk2v(f32x2 v) { return pk2(v[0], v[1]); }
DI float fmx(float a, float b) { return __builtin_elementwise_maximum(a, b); }
DI void sw32(float& a, float& b) { auto r = __builtin_amdgcn_permlane32_swap(__float_as_uint(a), __float_as_uint(b), false, false); a = __uint_as_float(r[0]); b = __uint_as_float(r[1]); }
DI void sw16(float& a, float& b) { auto r = __builtin_amdgcn_permlane16_swap(__float_as_uint(a), __float_as_uint(b), false, false); a = __uint_as_float(r[0]); b = __uint_as_float(r[1]); }
DI void vadd_s(unsigned& a, int s) { asm volatile("v_add_u32 %0, %1, %0" : "+v"(a) : "s"(s)); }
template <int LO> DI void glds(const void* sbase, unsigned voff, unsigned lds) {
  asm volatile("s_add_u32 m0, %2, %3\n\ts_nop 0\n\tglobal_load_lds_dwordx4 %0, %1" ::"v"(voff), "s"(sbase), "s"(lds), "i"(LO) : "memory", "scc");
}
DI void touch(const void* sbase, unsigned voff) { asm volatile("global_load_lds_dword %0, %1" ::"v"(voff), "s"(sbase) : "memory"); }
DI void gstore4_wt(void* sbase, unsigned voff, unsigned v) { asm volatile("global_store_dword %0, %1, %2 sc1" ::"v"(voff), "v"(v), "s"(sbase) : "memory"); }

#define NOPERM 0
__host__ DI int fperm(const void* Wg, int G, int i) {
  const int c = (3 - (int)((size_t)Wg >> 7) - G) & 7;
  return G * 9 + (NOPERM ? i : (i | 1) == c ? i ^ 1 : c == 0 && (i == 2 || i == 8) ? 10 - i : i);
}
namespace k4 {
#define K4_EO 0
constexpr int EO = K4_EO, UC = 98 + 2 * EO, UD = 126 + 2 * EO, UE = 162, NR = 41;
constexpr int slot_of(int u) { return u < 98 ? u % 7 : u >= UC && u < UD ? (u - UC) % 7 : 7 + (u & 1); }
constexpr int io_of(int u) { return u < 98 ? u / 7 : u < UC ? (u - 98) >> 1 : u < UD ? 14 + (u - UC) / 7 : (u - 126) >> 1; }
constexpr int ks_io(int i) { return i < 8 ? (i & 3) * 4 + (i >> 2) : i < 10 ? i + 8 : ((i - 2) & 3) * 4 + ((i - 2) >> 2); }
constexpr int ks_of(int u) { return ks_io(io_of(u)); }
}

namespace k1 {
constexpr int XB = 16384, SLOT = XB + 27648, EX = 3 * SLOT, ST = EX + 24576, VL = ST + 1024, PFD = VL + 4096, RDY = PFD + 256, LDSZ = RDY + 16, RMO = 73728;
constexpr unsigned MG0 = 0x6b8f31d7u, MG1 = 0x1ce4a59bu;
#define K1PRE 7
#define K1SP 11
#define K1TOP 0
#define K1DOT 0
#define K1SA 0
#define HTOP 1
#define K1RD 5
#define K1_N0 1
#define K2ROT 0
#define K2N0 6
#define K2NS 3
#define K2E 0
#define K1FT 2
#define K1H 1
#define HG 16
#define HF0 420
#define H18 0
}
DI void k1_pf(const u16* x, const u16* Wq, int xcd, int g, int s, int lane) {
  const unsigned l = min(lane, 29), r = l >= 15, c = l - 15 * r;
  const unsigned ow = __umul24(g * 64 + s * 2 + r, 3456) + ((c * 13) >> 6) * 512 + c * 128 + (xcd & 1) * 512;
  const int tok = min(max(((xcd >> 1) * 19 >> 2) * 112 - 16 + s * 22 + min(lane - 30, 21), 0), S_ - 1);
  unsigned o = lane < 30 ? ow : __umul24(tok, D_ * 2) + g * 128; opq_v(o);
  const char* p = (lane < 30 ? (const char*)Wq : (const char*)x) + o;
  asm volatile("global_load_lds_dword %0, off" ::"v"(p) : "memory");
}
DI bool empty8(const u32x4& a, const u32x4& b) {
  u64 m; unsigned t, u;
  asm volatile("v_max3_u32 %1, %3, %4, %5\n\tv_max3_u32 %2, %6, %7, %8\n\tv_max3_u32 %1, %1, %9, %10\n\tv_max_u32 %1, %1, %2\n\tv_cmp_eq_u32 %0, -1, %1"
               : "=s"(m), "=&v"(t), "=&v"(u) : "v"(a[0]), "v"(a[1]), "v"(a[2]), "v"(a[3]), "v"(b[0]), "v"(b[1]), "v"(b[2]), "v"(b[3]));
  return m != 0;
}
DI bool empty1(unsigned a) { u64 m; asm volatile("v_cmp_eq_u32 %0, -1, %1" : "=s"(m) : "v"(a)); return m != 0; }
DI void k1_wait(u32x2& f, const char* fb, unsigned fo) {
#pragma unroll 1
  for (int spin = 0; __builtin_amdgcn_ballot_w64(f[0] != k1::MG0 || f[1] != k1::MG1) != 0 && spin < (1 << 18); ++spin)
    asm volatile("s_sleep 1\n\tglobal_load_dwordx2 %0, %1, %2 sc1\n\ts_waitcnt vmcnt(0)" : "+v"(f) : "v"(fo), "s"(fb) : "memory");
}

typedef __attribute__((ext_vector_type(2))) u64 u64x2;
#define KARG(T, i) (*(const volatile __attribute__((address_space(4))) T*)((const __attribute__((address_space(4))) char*)__builtin_amdgcn_kernarg_segment_ptr() + 8 * (i)))
__global__ __launch_bounds__(768, 3) void k1_attn(const u16*, const u16*, const u16*, char*, const u16* Wg, const u16* Wu, const u16* Wd) {
  using namespace k1;
  __shared__ __attribute__((aligned(1024))) char smem[LDSZ];
  const u64x2 ka = KARG(u64x2, 0), kb = KARG(u64x2, 2);
  const u16* x = (const u16*)ka[0]; const u16* Wq = (const u16*)ka[1]; const u16* Wo = (const u16*)kb[0]; char* ws = (char*)kb[1];
  STAMP_BEGIN();
  const unsigned sb = (unsigned)(size_t)smem;
  const int w = __builtin_amdgcn_readfirstlane(threadIdx.x >> 6);
  const int lane = threadIdx.x & 63, g = lane >> 4, c = lane & 15;
  const int b = blockIdx.x, xcd = b & 7, bi = b >> 3;
  dma_setr(sb + PFD);
  if (w == 11) {
    u64 pc; unsigned len;
    asm volatile("s_getpc_b64 %0\n.Lpf_here:\n\ts_mov_b32 %1, (k_text1-.Lpf_here)&4294967295" : "=s"(pc), "=s"(len));
    touch((const void*)pc, min((unsigned)(bi + 32 * lane) * 128u, len - 4));
  }
  { const int n1 = HTOP && bi >= 19 && bi < 31 ? 13 + bi + 12 * w : 288;
#pragma unroll 1
    for (int n = w == 4 ? bi : n1; n < 288; n = n < 32 ? n1 : n + 144) k1_pf(x, Wq, xcd, n >> 5, n & 31, lane); }
  const int u = (xcd >> 1) * 19 + bi, tile = u >> 2, hg = (xcd & 1) * 4 + (u & 3), T0 = min(112 * tile, 1936);
  const int cg = bi >= 9 ? min(bi - 9, 8) : bi;
  if (__builtin_expect(w < 4 && bi < 19, 1)) {
    __builtin_amdgcn_s_setprio(3);
    unsigned vx[4], vw[7];
    const char* xs = (const char*)x; const char* wsrc = (const char*)Wq;
    unsigned dx = sb + w * 4096, dw = sb + XB + w * 7168, dl = dw - (w == 3 ? 1024 : 0);
    u64 em = (w & 1) ? ~0ull : ~0xffull;
    int step = SLOT;
    u32x4 one = {0x3f803f80u, 0x3f803f80u, 0x3f803f80u, 0x3f803f80u}, X[4] = {};
    f32x4 as[2] = {}, aq[2] = {};
    auto stat = [&](auto ic) AINL { constexpr int i = decltype(ic)::v;
      if constexpr ((i & 1) && (K1DOT & 1)) { for (int e = 0; e < 4; ++e) { float t = aq[(i >> 1) & 1][e]; a_dot(t, X[i >> 1][e], X[i >> 1][e]); aq[(i >> 1) & 1][e] = t; } }
      else if constexpr (!(i & 1) && (K1DOT & 2)) { for (int e = 0; e < 4; ++e) { float t = as[(i >> 1) & 1][e]; a_dot(t, X[i >> 1][e], one[0]); as[(i >> 1) & 1][e] = t; } }
      else if constexpr (i & 1) mf(aq[(i >> 1) & 1], X[i >> 1], X[i >> 1]); else mf(as[(i >> 1) & 1], one, X[i >> 1]); };
    auto sum4 = [&](const f32x4& v) AINL { float a = (v[0] + v[1]) + (v[2] + v[3]), t = a; sw32(a, t); a += t; t = a; sw16(a, t); return a + t; };
    unsigned sa, sa2;
    int sl = 0;
    auto rdx = [&]() AINL {
      rd128<0>(X[0], sa); rd128<2048>(X[1], sa); rd128<0>(X[2], sa2); rd128<2048>(X[3], sa2);
      const int st = sl == 2 ? -2 * SLOT : SLOT;
      vadd_s(sa, st); vadd_s(sa2, st);
      sl = sl == 2 ? 0 : sl + 1; };
    auto addr = [&](auto ic) AINL { constexpr int i = decltype(ic)::v;
      int ln = lane; if constexpr (i >= K1PRE) opq_v(ln);
      if constexpr (i < 4) { const int r = (w & 1) * 32 + 8 * i + (ln >> 3);
        vx[i] = __umul24(max(T0 - 16 + (w >> 1) + 2 * r, 0), D_ * 2) + ((ln ^ (r >> 1)) & 7) * 16; opq_v(vx[i]); }
      else if constexpr (i < 11) { const unsigned L = min(w * 7 + i - 4, 26) * 64 + ln, r = __umul24(L, 2428) >> 16, cc = L - r * 27;
        vw[i - 4] = __umul24((r & 56) + (r & 1) * 4 + ((r >> 1) & 3), 3456) + (__umul24(__umul24(cc, 57) >> 9, 1008) + (cc * 16 + hg * 144)); opq_v(vw[i - 4]); } };
    auto piece = [&](auto ic) AINL { constexpr int i = decltype(ic)::v;
      if constexpr (i == 0) asm volatile("s_mov_b64 exec, %3\n\ts_mov_b32 m0, %2\n\ts_nop 0\n\tglobal_load_lds_dwordx4 %0, %1\n\ts_mov_b64 exec, -1" ::"v"(vx[0]), "s"(xs), "s"(dx), "s"(em) : "memory");
      else if constexpr (i < 4) glds<i * 1024>(xs, vx[i], dx);
      else if constexpr (i < 10) glds<(i - 4) * 1024>(wsrc, vw[i - 4], dw);
      else glds<6144>(wsrc, vw[6], dl); };
    auto adv = [&]() AINL { xs += 128; wsrc += 64 * 3456; dx += step; dw += step; dl += step; };
    auto issue = [&]() AINL {
      sfor<0, 17>([&](auto ic) AINL { constexpr int i = decltype(ic)::v;
        if constexpr (i < 11) piece(ic);
        if constexpr (i >= K1SA && i < K1SA + 4) { stat(IC<2 * (i - K1SA)>{}); stat(IC<2 * (i - K1SA) + 1>{}); }
        if constexpr (i == K1SA + K1RD) { if constexpr (i > 11) asm volatile("s_nop 7"); rdx(); } });
      adv();
    };
    sfor<0, K1PRE>(addr);
    opq_s(dx); opq_s(dw); opq_s(dl); opq_s(em);
    STAMP_MID(1);
    opq_s(xs); opq_s(wsrc);
    sfor<0, K1PRE>(piece);
    sfor<K1PRE, 11>([&](auto ic) AINL { addr(ic); piece(ic); });
    adv();
    STAMP_MID(2);
    if (!(w & 1) && lane < 24) *LDSP(u32x4, sb + (lane >> 3) * SLOT + w * 4096 + (lane & 7) * 16) = u32x4{0x3f803f80u, 0x3f803f80u, 0x3f803f80u, 0x3f803f80u};
    *LDSP(unsigned, sb + RDY + w * 4) = 0u;
    sfor<0, K1SP>(piece);
    sa = sb + (32 * w + c) * 128 + (g ^ (c >> 1)) * 16; sa2 = sa ^ 64;
    opq_v(sa); opq_v(sa2); opq_v(one); opq_v(as[0]); opq_v(as[1]); opq_v(aq[0]); opq_v(aq[1]); opq_v(X[0]); opq_v(X[1]); opq_v(X[2]); opq_v(X[3]);
    STAMP_MID(3);
    wait_vl2<K1SP, 0>();
    STAMP_MID(4);
    abar();
    sfor<K1SP, 11>(piece);
    adv();
#pragma unroll 1
    for (int it = 0; it < 7; ++it) {
      step = sl == 0 ? -2 * SLOT : SLOT;
      issue();
      asm volatile("s_waitcnt vmcnt(11) lgkmcnt(0)" : "+v"(X[0]), "+v"(X[1]), "+v"(X[2]), "+v"(X[3])::"memory");
      abar();
    }
    const int lane_ = lane;
    int lane = lane_; opq_v(lane);
    unsigned v0, v1, fo = (lane < 8 ? 56 + lane : 14 * w - 8 + min(lane, 21)) * 2048 + b * 8, ro;
    { const int p = w * 8 + (lane >> 3), gq = (p >> 2) & 3, e = 4 * (p >> 4) + (p & 3), hd = 4 * (e >> 1) + gq;
      const unsigned co = ((((lane >> 1) & 3) ^ ((p >> 1) & 3)) * 32) + (lane & 1) * 16 + cg * 128;
      v0 = (10 * (gq & 2) + 8 * (gq & 1) + e) * (D_ * 2) + co; v1 = (18 * hd + (~hd & 1) * 16 + (e & 1)) * (D_ * 2) + co;
      const unsigned q = min(lane, 35);
      ro = (w >> 1) * (D_ * 2) + (q < 32 ? (w & 1) * 32 + ((q + 4 * cg) & 31) : (w & 1) * 4 + 32 + q) * 16; }
    opq_v(v0); opq_v(v1); opq_v(fo); opq_v(ro);
    auto wo = [&](auto ic) AINL { constexpr int i = decltype(ic)::v;
      glds<i * 4096>((const char*)Wo + (288 * (i / 9) + (i % 9 < 8 ? ((i % 9 + cg) & 7) * 36 : 0)) * (D_ * 2), i % 9 < 8 ? v0 : v1, sb + w * 1024); };
    u32x2 fl; u32x4 rmv;
    sfor<0, 10>([&](auto ic) AINL { constexpr int i = decltype(ic)::v; wo(ic);
      if constexpr (i < 4) { stat(IC<2 * i>{}); stat(IC<2 * i + 1>{}); }
      if constexpr (i == 4) rdx(); });
    asm volatile("s_waitcnt vmcnt(10) lgkmcnt(0)" : "+v"(X[0]), "+v"(X[1]), "+v"(X[2]), "+v"(X[3])::"memory");
    abar();
    gload8_c<0>(fl, ws + WS_FR, fo);
    sfor<0, 8>(stat);
    rdx();
    asm volatile("s_waitcnt lgkmcnt(0)" : "+v"(X[0]), "+v"(X[1]), "+v"(X[2]), "+v"(X[3])::"memory");
    sfor<0, 8>(stat);
    asm volatile("s_nop 7\n\ts_nop 3" : "+v"(as[0]), "+v"(as[1]), "+v"(aq[0]), "+v"(aq[1]));
    for (int i = 0; i < 2; ++i) {
      const float m = ((K1DOT & 2) ? sum4(as[i]) : as[i][0]) * (1.f / D_), q = (K1DOT & 1) ? sum4(aq[i]) : (c & 2) ? ((c & 1) ? aq[i][3] : aq[i][2]) : ((c & 1) ? aq[i][1] : aq[i][0]);
      const float rs = __builtin_amdgcn_rsqf(__builtin_fmaxf(__builtin_fmaf(-m, m, q * (1.f / D_)), 0.f) + 1e-5f);
      if (g == c >> 2) { *LDSP(float, sb + ST + (32 * w + 16 * i + c) * 4) = -m; *LDSP(float, sb + ST + 512 + (32 * w + 16 * i + c) * 4) = rs; }
    }
    wait_vl2<63, 0>();
    abar();
    sfor<10, 18>(wo);
    asm volatile("s_waitcnt vmcnt(8)" : "+v"(fl)::"memory");
    k1_wait(fl, ws + WS_FR, fo);
    *LDSP(unsigned, sb + RDY + w * 4) = 1u;
    gload16_c<0>(rmv, ws + WS_RM, ro);
    asm volatile("s_waitcnt vmcnt(0)" : "+v"(rmv)::"memory");
    if (lane < 36) *LDSP(u32x4, sb + RMO + w * 576 + lane * 16) = rmv;
    wait_vl2<63, 0>();
    abar();
    if (lane < 22 && (lane >= 8 || !w)) gstore8_wt<0>(ws + WS_FR, fo, u32x2{0u, 0u});
    STAMP_END(ws, 0);
    return;
  }
  const int rrow = g * 4 + (c >> 2);
  const u32x4 ones = {~0u, ~0u, ~0u, ~0u};
  auto pfill = [&](int e, int e1, int st) AINL {
    for (; e < e1; e += st) { const int blk = (e * 2428) >> 16; gstore16_wt<0>(ws + WS_PART + (size_t)(blk * 8 + xcd) * PART_BLK + (e - blk * 27) * 1024, lane * 16, ones); } };
  if (bi >= 19) {
    const int hi = bi - 19, hw = hi < 5 ? hi + 5 * w : hi - 5 + 8 * w;
    if (hi < 12) {
      if (!HTOP) for (int q = hi + 12 * w; q < 256; q += 144) k1_pf(x, Wq, xcd, 1 + (q >> 5), q & 31, lane);
      if (HG & 32) wait_vm<0>(); else if (HG & 64) wait_vm<1>();
      if (!(HG & 32)) abar();
      if (hi >= 5) for (int e = ((hi - 5) * 8 + xcd) * 12 + w; e < (int)(WS_HC >> 10); e += 672) gstore16_wt<0>(ws + e * 1024, lane * 16, ones);
    } else {
      u32x4 v[9];
      const unsigned r = xcd * 72 + min(w, 8) * 8 + (lane >> 3);
      sfor<0, 9>([&](auto ic) AINL { constexpr int i = decltype(ic)::v; gload16<i * 128>(v[i], Wo, r * (D_ * 2) + (lane & 7) * 16); });
      asm volatile("s_waitcnt vmcnt(0)" : "+v"(v[0]), "+v"(v[1]), "+v"(v[2]), "+v"(v[3]), "+v"(v[4]), "+v"(v[5]), "+v"(v[6]), "+v"(v[7]), "+v"(v[8])::"memory");
      float t = 0.f;
      for (int i = 0; i < 36; ++i) t = dot2(v[i >> 2][i & 3], 0x3f803f80u, t);
      t = sum8(t) * (1.f / D_);
      const float hv = rbf(t);
      const unsigned hd = (r * 3641) >> 16, d = r - 18 * hd - 2 * (hd & 1), pos = d < 16 ? 16 * hd + d : 512 + 2 * (hd & 16) + 8 * (hd & 3) + ((hd >> 1) & 6) + (d & 1);
      if ((lane & 7) < 2 && w < 9) asm volatile("global_store_short %0, %1, %2 sc1" ::"v"((lane & 1) * (D_ * 2) + pos * 2), "v"(pk2((lane & 1) ? t - hv : hv, 0.f)), "s"(ws + WS_RM) : "memory");
      abar();
    }
#pragma unroll 1
    for (int ps = 0; ps < 2; ++ps) {
#pragma unroll 1
      for (int n = (hi * 8 + xcd) * 12 + w; n < 1296; n = hi == 12 && n < 1248 ? 1248 + w * 8 + xcd : 1296) {
        const int ksg = n / 9, cc = n - ksg * 9;
        const int q = ksg / 36, kk = ksg - q * 36, kh = kk >= 18, ks = kk - 18 * kh;
        const unsigned ub = sb + (w + (n >= 1248 ? 12 : 0)) * 4096;
        if (ps == 0) {
          const char* src = (const char*)Wd + (fperm(Wg, 2 * q + kh, ks >> 1) * 2 + (ks & 1)) * 32 * (D_ * 2) + cc * 128;
          sfor<0, 4>([&](auto ic) AINL { constexpr int i = decltype(ic)::v;
            const unsigned p = i * 8 + (lane >> 3), kn = 8 * ((p >> 2) & 3) + 4 * (p >> 4) + (p & 3);
            glds_nt<i * 1024>(src, (ks >= 16 ? kn : p) * (D_ * 2) + ((((lane >> 1) & 3) ^ ((p >> 1) & 3)) * 32) + (lane & 1) * 16, ub); });
        } else {
          const int io = ks >= 16 ? ks - 8 : (ks & 3) * 4 + (ks >> 2) + (ks & 2);
          u32x2 v[4][2];
          sfor<0, 4>([&](auto tc) AINL { constexpr int t = decltype(tc)::v;
            const unsigned a = ub + rrow * 128 + ((t ^ ((rrow >> 1) & 3)) * 32) + (lane & 3) * 8; trd<0>(v[t][0], a); trd<2048>(v[t][1], a); });
          sfor<0, 4>([&](auto tc) AINL { constexpr int t = decltype(tc)::v;
            const unsigned T = cc * 4 + t, cg = T / 9, lt = T - cg * 9 - (cg & 1), sl = (((lt >> 1) - q - 1) & 3) * 2 + (lt & 1);
            const unsigned u = lt >= 8 || sl < 6 ? 7 * io + (lt >= 8 ? 6 : sl) + (io >= 14 ? 2 * k4::EO : 0) : (io < k4::EO ? 92 : 120) + 2 * io + sl;
            asm volatile("s_waitcnt lgkmcnt(%2)" : "+v"(v[t][0]), "+v"(v[t][1]) : "i"(6 - 2 * t));
            gstore16_wt<0>(ws + WS_WDP + (size_t)((q * 4 + cg) * 324 + 2 * u + kh) * 1024, lane * 16, cat4(v[t][0], v[t][1])); });
        }
      }
      if (ps == 0) {
        if (hi == 12 && w < 6) wait_vm<8>(); else if (hi >= 5) wait_vm<4>();
        abar();
        if (hi >= 5) {
          if (w < 3) gstore8_wt<0>(ws + WS_FR + ((hi - 5) * 8 + xcd) * 2048 + w * 512, lane * 8, u32x2{MG0, MG1});
          abar();
        }
        if (HG & 16) wait_vm<HG & 15>();
        STAMP_MID(1);
        dma_setr(sb + PFD);
        const unsigned id = min(lane, 53), rr = (id * 3641) >> 16, o = rr * (F_ * 2 - 2304) + id * 128 + (xcd & 3) * 2304;
        for (int e = hi < 5 ? hw : 96 + hw; e < (hi < 5 ? 96 : 192); e += 60 + (hi >= 5) * 36) touch((const char*)(xcd & 4 ? Wu : Wg) + (size_t)e * (3 * F_ * 2), o);
        if (hi < 5 && hw < 36) wait_vm<2>(); else wait_vm<1>();
        STAMP_MID(2);
      }
    }
    if (hi < 4 && w == 0) gstore16_wt<0>(ws + WS_FG + (hi * 8 + xcd) * 1024, lane * 16, ones);
    if (hi < 5) pfill(hw, HF0, 60); else pfill(HF0 + hw, 864 - H18, 96);
    wait_vm<0>();
    STAMP_END(ws, 0);
    return;
  }
  const int kh = w & 1, tb = (2 * xcd + (bi >= 9)) * 8 + ((w - 4 + 2 * K2ROT * cg) & 7);
  int j = tb - min(((tb * 147) >> 10) * 7, 121);
  const char* xrb = (const char*)x + (unsigned)((16 * tb - ((0x54 >> j) & 1) * 15) * (D_ * 2) + cg * 128);
  const char* ob[8];
  for (int k = 0; k < 8; ++k) { ob[k] = ws + (unsigned)((tb & ~1) * 16384 + (kh * 8 + ((k + cg) & 7)) * 1024); opq_s(ob[k]); }
  const char* lb = ws + ((unsigned)WS_OL + (tb & ~1) * OLB + kh * 1024);
  char* hb = ws + ((unsigned)WS_HC + (tb * 18 + cg * 2) * 1024); char* qb = ws + ((unsigned)WS_SSQ + cg * 8192 + tb * 64);
  unsigned sc[9] = {sb + kh * 36864, sb + RMO + kh * 576, 0, 0, kh * 16384u, kh * (unsigned)OLB, kh * (OLB - 960u),
                    sb + 81920 + w * 5120, sb + 81920 + (w ^ 1) * 5120};
  for (int k = 0; k < 9; ++k) if (k != 2 && k != 3) opq_s(sc[k]);
  u32x4 rsO = mkrsrc(ws, WS_HC);
  opq_s(j); opq_s(xrb); opq_s(lb); opq_s(hb); opq_s(qb); opq_s(rsO);
  __builtin_amdgcn_s_setprio(1);
  {
  const int h = w & 3, par = (w >> 2) - 1, f = h & 1;
  unsigned xa = sb + (par * 64 + c) * 128 + ((g ^ (c >> 1)) * 16 ^ f * 64), xb = xa ^ 64;
  const unsigned wr = sb + XB + (8 * g + 2 * (c >> 2)) * 432;
  unsigned wa = wr + (c & 3) * 8 + 36 * h + 4 * f + f * 13824, wb = wa + 13824 - f * 27648;
  unsigned la = wr + f * 13824 + 32 + 72 * (h >> 1) + ((c & 2) ? 288 : (c & 1) ? 0 : 144);
  unsigned ea = sb + EX + (w - 4) * 3072 + min(g, 2) * 256 + c * 16;
  f32x4 acc[4][4], bias[3];
  unsigned mk[2];
  for (int i = 0; i < 16; ++i) { acc[i >> 2][i & 3] = f32x4{0.f, 0.f, 0.f, 0.f}; opq_v(acc[i >> 2][i & 3]); }
  for (int r = 0; r < 4; ++r) { const int d = c - 4 * g - r; const float ninf = -__builtin_inff();
    bias[1][r] = d >= 0 && d < 8 ? 0.f : ninf; bias[2][r] = d < -8 ? 0.f : ninf; bias[0][r] = (T0 == 0 && g < 2) ? ninf : bias[1][r]; }
  for (int e = 0; e < 2; ++e) { const int d = c - 4 * g - 2 * e; mk[e] = d > 0 ? ~0u : d == 0 ? 0xffffu : 0u; opq_v(mk[e]); }
  const unsigned vl = sb + VL + (w - 4) * 512;
  *LDSP(unsigned, vl + lane * 8 + 4) = 0x3f803f80u;
  unsigned va = g == 2 ? vl + c * 8 : sb + 2 * SLOT + lane * 8, so[4];
  { const unsigned off = 1u << 31, t7 = min(7 * tile, 121), o0 = t7 * 16384 + (hg * 4 + h) * 512 + (g >> 1) * 256 + c * 16, l18 = tile == 18 ? off : 0u;
    so[1] = (o0 + (3 + 2 * (g & 1) + par) * 16384) | (l18 & ~(lane << 27));
    so[0] = (o0 + ((g & 1) ? (1 + par) * 16384 : par * 128 - 128)) | ((lane & 24) ? l18 : off);
    so[2] = ((unsigned)WS_OL + t7 * OLB + (hg * 4 + h) * 64 + (g ? __umul24(2 * g - 1 + par, OLB) : par * 32 - 32) + c * 4) | (lane >= (tile == 18 ? 48 : 8) ? 0u : off);
    so[3] = (so[2] + 2048 - hg * 240) | (h | (hg & 3) ? off : 0u);
  }
  opq_v(va); opq_v(so[0]); opq_v(so[1]); opq_v(so[2]); opq_v(so[3]);
  opq_v(bias[0]); opq_v(bias[1]); opq_v(bias[2]); opq_v(xa); opq_v(xb); opq_v(wa); opq_v(wb); opq_v(la); opq_v(ea);
  u32x4 X0[4], X1[4];
  u32x2 W0[4][2], W1[4][2];
  auto rd0 = [&](auto ic) AINL { constexpr int i = decltype(ic)::v;
    if constexpr (i == 0) rd128<0>(X0[0], xa);
    else if constexpr (i < 3) trd<(i - 1) * 432>(W0[0][i - 1], la);
    else if constexpr (i < 6) rd128<(i - 2) * 2048>(X0[i - 2], xa);
    else trd<((i - 6) >> 1) * 144 + (i & 1) * 432>(W0[1 + ((i - 6) >> 1)][i & 1], wa); };
  auto rd1 = [&](auto ic) AINL { constexpr int i = decltype(ic)::v;
    if constexpr (i < 4) rd128<i * 2048>(X1[i], xb);
    else trd<((i - 4) >> 1) * 144 + (i & 1) * 432>(W1[1 + ((i - 4) >> 1)][i & 1], wb); };
  auto mfB = [&](auto mc) AINL { constexpr int t = 1 + decltype(mc)::v / 4, n = decltype(mc)::v & 3;
    if constexpr (t < 3) mf(acc[t][n], cat4(W1[t][0], W1[t][1]), X1[n]); else mf(acc[t][n], X1[n], cat4(W1[t][0], W1[t][1])); };
  int sl = 0;
  auto phA = [&](auto lc) AINL {
    const int st = sl == 2 ? -2 * SLOT : SLOT;
    sl = sl == 2 ? 0 : sl + 1;
    sfor<0, 16>([&](auto mc) AINL { constexpr int m = decltype(mc)::v, t = m >> 2, n = m & 3;
      if constexpr (m < 4) wait_vl2<63, 7>();
      if constexpr (m == 4) wait_vl2<63, 6>();
      if constexpr (m == 8) wait_vl2<63, 8>();
      if constexpr (m == 12) wait_vl2<63, 10>();
      if constexpr (t < 3) mf(acc[t][n], cat4(W0[t][0], W0[t][1]), X0[n]); else mf(acc[t][n], X0[n], cat4(W0[t][0], W0[t][1]));
      if constexpr (m < 2) rd0(IC<10 + m>{});
      if constexpr (m >= 2 && m < 12) rd1(IC<m - 2>{});
      if constexpr (!decltype(lc)::v) {
        if constexpr (m == 12) { vadd_s(xa, st); vadd_s(la, st); }
        if constexpr (m == 13) { vadd_s(wa, st); vadd_s(xb, st); }
        if constexpr (m == 14) vadd_s(wb, st);
      } else if constexpr (m >= 12) wr128<(m - 12) * 768>(ea, acc[0][m - 12]);
    });
    asm volatile("s_waitcnt lgkmcnt(0)" : "+v"(X1[0]), "+v"(X1[1]), "+v"(X1[2]), "+v"(X1[3]), "+v"(W1[1][0]), "+v"(W1[1][1]), "+v"(W1[2][0]), "+v"(W1[2][1]), "+v"(W1[3][0]), "+v"(W1[3][1])::"memory");
    abar();
  };
  STAMP_MID(5);
  abar();
  STAMP_MID(1);
  sfor<0, 10>(rd0);
#pragma unroll 1
  for (int it = 0; it < 8; ++it) {
    phA(IC<0>{});
    sfor<0, 12>([&](auto mc) AINL { constexpr int m = decltype(mc)::v; mfB(mc); if constexpr (m < 10) rd0(mc); });
  }
  phA(IC<1>{});
  STAMP_MID(2);
  unsigned nm;
  f32x2 sT[4], lf[4][2];
  f32x4 sM[4], sR[4];
  { unsigned e0 = ea + f * 8, e1 = e0 + (f ? -3072 : 3072), s0 = sb + ST + (par * 64 + c) * 4, s1 = sb + ST + (par * 64 + 4 * g) * 4;
    unsigned s2 = s0 + g * 64;
    opq_v(e0); opq_v(e1); opq_v(s0); opq_v(s1); opq_v(s2);
    sfor<0, 12>([&](auto mc) AINL { constexpr int m = decltype(mc)::v; mfB(mc);
      if constexpr (m < 4) { rd2f<m * 16, 128 + m * 16>(sT[m], s0); rd64f<m * 768>(lf[m][0], e0); rd64f<m * 768>(lf[m][1], e1); }
      else if constexpr (m < 8) { rd128f<(m - 4) * 64>(sM[m - 4], s1); rd128f<(m - 4) * 64 + 512>(sR[m - 4], s1); }
      else if constexpr (m == 8) rd32(nm, s2); }); }
  asm volatile("s_waitcnt lgkmcnt(0)" : "+v"(sT[0]), "+v"(sT[1]), "+v"(sT[2]), "+v"(sT[3]), "+v"(lf[0][0]), "+v"(lf[0][1]), "+v"(lf[1][0]), "+v"(lf[1][1]), "+v"(lf[2][0]), "+v"(lf[2][1]), "+v"(lf[3][0]), "+v"(lf[3][1]),
               "+v"(sM[0]), "+v"(sM[1]), "+v"(sM[2]), "+v"(sM[3]), "+v"(sR[0]), "+v"(sR[1]), "+v"(sR[2]), "+v"(sR[3]), "+v"(acc[1][0]), "+v"(acc[1][1]), "+v"(acc[1][2]), "+v"(acc[1][3]), "+v"(nm)::"memory");
  u32x4 Kf[4], Qf[4];
  { f32x2 l[4];
    for (int n = 0; n < 4; ++n) l[n] = lf[n][0] + lf[n][1];
    const f32x2 cs = {bc0(l[0][0]), bc0(l[0][1])};
    sfor<0, 4>([&](auto nc) AINL { constexpr int n = decltype(nc)::v;
      const float fc = (g & 1) ? (g == 1 ? 1.f : 0.f) : sT[n][1];
      const unsigned p = pk2v(fma2(sp2(sT[n][0]), cs, l[n]) * sp2(fc));
      wr32u<n * 128>(va, p);
      auto r = __builtin_amdgcn_permlane16_swap(p, 0u, false, false);
      Kf[n][2] = r[0]; Qf[n][2] = r[1]; Kf[n][3] = 0u; Qf[n][3] = 0u; }); }
  u32x8 VL8;
  { unsigned a = vl + (4 * g + (c >> 2)) * 8; u32x2 t[4];
    trd<0>(t[0], a); trd<128>(t[1], a); trd<256>(t[2], a); trd<384>(t[3], a);
    VL8 = u32x8{t[0][0], t[0][1], t[1][0], t[1][1], t[2][0], t[2][1], t[3][0], t[3][1]}; }
  for (int e = 0; e < 2; ++e) { const f32x2 cs = {bc0(acc[1][0][2 * e]), bc0(acc[1][0][2 * e + 1])};
    for (int n = 3; n >= 0; --n) Qf[n][e] = pk2v(fma2(sp2(sT[n][0]), cs, h2(acc[1][n], e))); }
  asm volatile("" : "+v"(acc[2][0]), "+v"(acc[2][1]), "+v"(acc[2][2]), "+v"(acc[2][3]), "+v"(Qf[0]));
  for (int e = 0; e < 2; ++e) { const f32x2 cs = {bc0(acc[2][0][2 * e]), bc0(acc[2][0][2 * e + 1])};
    for (int n = 3; n >= 0; --n) Kf[n][e] = pk2v(fma2(sp2(sT[n][0]), cs, h2(acc[2][n], e)) * sp2(sT[n][1])); }
  f32x4 S[4];
  float mx[4];
  for (int n = 0; n < 4; ++n) {
    S[n] = mfb(Kf[n], Qf[n], bias[n > 0]);
    if (n) S[n] = __builtin_elementwise_maximum(S[n], mfb(Kf[n - 1], Qf[n], bias[2]));
    mx[n] = fmx(fmx(S[n][0], S[n][1]), fmx(S[n][2], S[n][3]));
  }
  asm volatile("" : "+v"(acc[3][0]), "+v"(acc[3][1]), "+v"(acc[3][2]), "+v"(acc[3][3]), "+v"(Kf[0]));
  u32x8 V8;
  { float cs = acc[3][0][0], t = cs; sw16(cs, t); t = cs; sw32(cs, t);
    for (int n = 0; n < 4; ++n) for (int e = 0; e < 2; ++e) V8[2 * n + e] = pk2v(fma2(h2(sM[n], e), sp2(cs), h2(acc[3][n], e)) * h2(sR[n], e)); }
  sw32(mx[0], mx[1]); sw32(mx[2], mx[3]);
  mx[0] = fmx(mx[0], mx[1]); mx[2] = fmx(mx[2], mx[3]);
  sw16(mx[0], mx[2]);
  mx[0] = mx[2] = fmx(mx[0], mx[2]);
  sw16(mx[0], mx[2]);
  mx[1] = mx[0]; mx[3] = mx[2];
  sw32(mx[0], mx[1]); sw32(mx[2], mx[3]);
  asm volatile("s_waitcnt lgkmcnt(0)" : "+v"(VL8), "+v"(V8)::"memory");
  u32x2 om[4]; float olf[4];
  sfor<0, 4>([&](auto nc) AINL { constexpr int n = decltype(nc)::v, n0 = n ? 2 * n - 2 : 0;
    const float a = sT[n][1] * (0.23570226039551584f * 1.44269504088896f), ma = -mx[n] * a;
    const f32x2 x0 = fma2(lo2(S[n]), sp2(a), sp2(ma)), x1 = fma2(hi2(S[n]), sp2(a), sp2(ma));
    const unsigned p0 = pk2(__builtin_amdgcn_exp2f(x0[0]), __builtin_amdgcn_exp2f(x0[1])), p1 = pk2(__builtin_amdgcn_exp2f(x1[0]), __builtin_amdgcn_exp2f(x1[1]));
    const u32x4 P = n ? u32x4{p0 & ~mk[0], p1 & ~mk[1], p0 & mk[0], p1 & mk[1]} : u32x4{p0, p1, 0u, 0u};
    const f32x4 z = {0.f, 0.f, 0.f, 0.f}, o = mfb(__builtin_shufflevector(V8, V8, n0, n0 + 1, n0 + 2, n0 + 3), P, z), l = mfb(__builtin_shufflevector(VL8, VL8, n0, n0 + 1, n0 + 2, n0 + 3), P, z);
    const f32x2 inv = sp2(__builtin_amdgcn_rcpf(l[3]));
    om[n] = u32x2{pk2v(lo2(o) * inv), pk2v(hi2(o) * inv)}; olf[n] = __uint_as_float(pk2v(lo2(l) * inv));
  });
  { unsigned f, n;
    asm volatile("s_mov_b32 %1, 0x100000\n0:\n\tds_read_b32 %0, %3\n\ts_waitcnt lgkmcnt(0)\n\tv_cmp_eq_u32 vcc, 0, %0\n\ts_cbranch_vccz 1f\n\ts_sub_u32 %1, %1, 1\n\ts_cbranch_scc0 0b\n1:"
                 : "=&v"(f), "=&s"(n), "+v"(om[0]) : "v"(sb + RDY + g * 4) : "memory", "vcc", "scc"); }
  bstore4(rsO, so[3], nm);
  { for (int i = 0; i < 2; ++i) {
      auto r = __builtin_amdgcn_permlane16_swap(om[2 * i][0], om[2 * i + 1][0], false, false), q = __builtin_amdgcn_permlane16_swap(om[2 * i][1], om[2 * i + 1][1], false, false);
      bstore16(rsO, so[i], u32x4{r[0], q[0], r[1], q[1]});
    }
    sw16(olf[0], olf[1]); sw16(olf[2], olf[3]); sw32(olf[0], olf[2]);
    bstore4(rsO, so[2], __float_as_uint(olf[0])); }
  }
  STAMP_MID(3);
  __builtin_amdgcn_s_setprio(0);
  u32x4 X[18], xr[2];
  unsigned mx, XL[8];
  unsigned xro = tb_tok(j, c) * (D_ * 2) + g * 16; opq_v(xro);
  unsigned ra[4];
  for (int t = 0; t < 4; ++t) ra[t] = sc[0] + rrow * 128 + ((t ^ ((rrow >> 1) & 3)) * 32) + (lane & 3) * 8;
  unsigned rm = sc[1] + (lane & 1) * (D_ * 2) + g * 16;
  opq_v(ra[0]); opq_v(ra[1]); opq_v(ra[2]); opq_v(ra[3]); opq_v(rm);
  u32x4 s0, s1;
  { const unsigned one = 0x3f80u << (16 * (lane & 1)); const int d = (lane & 7) >> 1, hit = g - ((lane >> 3) & 1);
    const unsigned o0 = hit == 0 ? one : 0u, o1 = hit == 2 ? one : 0u;
    for (int i = 0; i < 4; ++i) { s0[i] = i == d ? o0 : 0u; s1[i] = i == d ? o1 : 0u; }
    opq_v(s0); opq_v(s1); }
  const unsigned l16 = lane * 16;
  unsigned xo[2] = {l16 + sc[4], l16 + (sc[4] ^ 16384u)}, lo[3] = {lane * 4 + sc[5], lane * 4 + (sc[5] ^ (unsigned)OLB), c * 4 + sc[6]};
  unsigned ew = sc[7] + l16, er = sc[8] + l16, qo = c * 4 + (lane < 16 - 16 * min(cg, 1) ? 9 * 8192 : 0); opq_v(qo);
  opq_v(xo[0]); opq_v(xo[1]); opq_v(lo[0]); opq_v(lo[1]); opq_v(lo[2]); opq_v(ew); opq_v(er);
  if (bi == 18) { abar(); pfill(864 - H18 + w - 4, 864, 8); STAMP_END(ws, 0); return; }
  gload16<0>(xr[0], xrb, xro); gload16<64>(xr[1], xrb, xro);
  auto ld2 = [&](auto kc) AINL { constexpr int k = decltype(kc)::v;
    if constexpr (k < 16) gload16_c<0>(X[k], ob[k >> 1], xo[k & 1]);
    else if constexpr (k < 24) gload4_c<(k & 3) * 256>(XL[k - 16], lb, lo[~k >> 2 & 1]);
    else if constexpr (k == 24) gload4_c<2048>(mx, lb, lo[2]); };
  auto re2 = [&](auto kc) AINL { constexpr int k = decltype(kc)::v;
    if constexpr (k < 16) gload16_d(X[k], ob[k >> 1], xo[k & 1]);
    else if constexpr (k < 24) gload4_d<(k & 3) * 256>(XL[k - 16], lb, lo[~k >> 2 & 1]);
    else if constexpr (k == 24) gload4_d<2048>(mx, lb, lo[2]); };
  sfor<0, K2E>(ld2);
  abar();
  STAMP_MID(4);
  sfor<K2E, K2N0>(ld2);
  u32x2 A[2][4][2];
  u32x4 Ar[2];
  auto rdA = [&](auto kc, auto tc) AINL { constexpr int k = decltype(kc)::v, t = decltype(tc)::v;
    if constexpr (k < 9 && t < 4) { trd<k * 4096>(A[k & 1][t][0], ra[t]); trd<k * 4096 + 2048>(A[k & 1][t][1], ra[t]); }
    if constexpr (k < 9 && t == 4) rd128<k * 64>(Ar[k & 1], rm); };
  sfor<0, 10>([&](auto ic) AINL { constexpr int i = decltype(ic)::v; rdA(IC<i / 5>{}, IC<i % 5>{}); });
  f32x4 acc[2][5];
  asm volatile("s_waitcnt vmcnt(%2)" : "+v"(xr[0]), "+v"(xr[1]) : "i"(K2N0) : "memory");
  for (int t = 0; t < 4; ++t) mf0(acc[0][t], (t & 1) ? s1 : s0, xr[t >> 1]);
  int spin = 1 << 16;
  sfor<0, 8>([&](auto kc) AINL {
    constexpr int k = decltype(kc)::v, n = K2N0 + K2NS * k < 25 ? K2N0 + K2NS * k : 25;
    asm volatile("s_waitcnt vmcnt(%2)" : "+v"(X[2 * k]), "+v"(X[2 * k + 1]) : "i"(n - 2 - 2 * k) : "memory");
    if (__builtin_expect(empty8(X[2 * k], X[2 * k + 1]), 0)) {
#pragma unroll 1
      do { sfor<2 * k, n>(re2); wait_vm<0>(); } while (empty8(X[2 * k], X[2 * k + 1]) && --spin > 0);
    }
    sfor<0, 5>([&](auto tc) AINL {
      constexpr int t = decltype(tc)::v;
      sfor<0, 2>([&](auto ic) AINL {
        constexpr int i = decltype(ic)::v;
        wait_vl2<63, (i ? 15 : k < 7 ? 14 : t < 4 ? (16 - 2 * t < 14 ? 16 - 2 * t : 14) : 9)>();
        const u32x4 a = t < 4 ? cat4(A[k & 1][t & 3][0], A[k & 1][t & 3][1]) : Ar[k & 1];
        if constexpr (k == 0 && (i || t == 4)) mf0(acc[i][t], a, X[i]); else mf(acc[i][t], a, X[2 * k + i]);
      });
      rdA(IC<k + 2>{}, tc);
      if constexpr (t < K2NS) ld2(IC<n + t>{});
    });
  });
  static_assert(K2N0 + 8 * K2NS >= 25 && K2N0 >= 2 && K2NS >= 2 && K2NS <= 5 && K2E <= K2N0);
  asm volatile("s_waitcnt vmcnt(0) lgkmcnt(0)" : "+v"(XL[0]), "+v"(XL[1]), "+v"(XL[2]), "+v"(XL[3]), "+v"(XL[4]), "+v"(XL[5]), "+v"(XL[6]), "+v"(XL[7]), "+v"(mx)::"memory");
  auto emptyL = [&]() AINL { return empty8(u32x4{XL[0], XL[1], XL[2], XL[3]}, u32x4{XL[4], XL[5], XL[6], XL[7]}) | empty1(mx); };
  if (__builtin_expect(emptyL(), 0)) {
#pragma unroll 1
    do { sfor<16, 25>(re2); wait_vm<0>(); } while (emptyL() && --spin > 0);
  }
  asm volatile("" : "+v"(XL[0]), "+v"(XL[1]), "+v"(XL[2]), "+v"(XL[3]), "+v"(XL[4]), "+v"(XL[5]), "+v"(XL[6]), "+v"(XL[7]), "+v"(mx));
  sfor<0, 2>([&](auto ic) AINL {
    constexpr int i = 1 - decltype(ic)::v, o = 4 - 4 * i;
    X[16 + i] = u32x4{XL[o], XL[o + 1], XL[o + 2], XL[o + 3]}; asm volatile("s_nop 1" : "+v"(X[16 + i]));
    sfor<0, 5>([&](auto tc) AINL {
      constexpr int t = decltype(tc)::v;
      mf(acc[i][t], t < 4 ? cat4(A[0][t & 3][0], A[0][t & 3][1]) : Ar[0], X[16 + i]);
      if constexpr (i == 0 && t < 4) wr128<t * 1024>(ew, acc[1][t]);
      if constexpr (i == 0 && t == 4) wr64f<4096>(ew, lo2(acc[1][4]));
    });
  });
  wait_vl2<63, 0>();
  abar();
  f32x4 rr[4]; f32x2 r4;
  rd64f<4096>(r4, er);
  sfor<0, 4>([&](auto tc) AINL { constexpr int t = decltype(tc)::v; rd128f<t * 1024>(rr[t], er); });
  STAMP_MID(6);
  asm volatile("s_waitcnt lgkmcnt(0)" : "+v"(acc[0][0]), "+v"(acc[0][1]), "+v"(acc[0][2]), "+v"(acc[0][3]), "+v"(acc[0][4]), "+v"(rr[0]), "+v"(rr[1]), "+v"(rr[2]), "+v"(rr[3]), "+v"(r4)::"memory");
  r4 += lo2(acc[0][4]);
  const f32x2 mean = sp2((r4[0] + r4[1]) - __uint_as_float(mx));
  u32x4 hc[2];
  f32x2 s2 = {0.f, 0.f};
  for (int t = 0; t < 4; ++t)
    for (int i = 0; i < 2; ++i) {
      const f32x2 d = (h2(acc[0][t], i) - mean) + h2(rr[t], i);
      hc[t >> 1][(t & 1) * 2 + i] = pk2v(d);
      s2 = fma2(d, d, s2);
    }
  gstore16_hc<0>(hb, l16, hc[0]); gstore16_hc<1024>(hb, l16, hc[1]);
  float ss = s2[0] + s2[1], st = ss;
  sw32(ss, st); ss += st; st = ss; sw16(ss, st); ss += st;
  gstore4_wt(qb, qo, __float_as_uint(qo > 64 ? mean[0] : ss));
  STAMP_END(ws, 0);
}

#define K3_PG -2, -2, -2, -2, -1, -1, -1, -1, -1, -2, -2, 2, 3, 6, 7
#define K4_UP2 -9
#define K4_AG 1
#define K4_PS 1
#define K4_SL 1
#define K4_ONE 1
#define K4_CAP 16
#define K4_BS 8
#define K4_UPL -1
#define K4_FB 209, 257, 273, 337, 369
#define K4_PQ 246, 278, 294, 358, 392, 424
#define K4_PD 12
#define K4_XS 6
#define K3_TU -1
#define K3_TN 1
#define K3_TS 8
#define K4_SKIP 0
#define K4_UH 112
#define K4_FI 30
#define K4_FS 16
#define K4_WS 4
#define EXPD 0
namespace k3 {
using namespace k4;
constexpr int NSLOT = 7, C2OFF = NSLOT * 16384;
constexpr int U1 = 144, U2 = 180, U3 = 252, U4 = 324, G4 = 2 * U4, NG = G4 + 2 * UE, UPL = K4_UPL;
enum { OA3 = 0, OA4, ORW, ORR, OF4, OV, OADD, OCVT, OCHK, OFLG, OS3, OSC, OS4, OW3, OC2, OX3, OSQ, OST, OW4, OX4, OH, OPS, OPL, OPF, OT3, OFI, NK };
enum { VM1 = 0, VEX, VFM, VRC, VM2, VCV, VPL };
constexpr int NO = 1700;
#define K4_ST 0, U1, U3, U4, U4 + UC
constexpr int stp(int u) { int a[5] = {K4_ST}; for (int i = 0; i < 5; ++i) if (a[i] == u) return i + 1; return 0; }
constexpr int w4seg(int r) { return r % 20 < 6 ? 112 : r % 20 < 12 ? 64 : 0; }
constexpr int w4off(int r) { return (r % 20 < 6 ? r % 20 : r % 20 < 12 ? r % 20 - 6 : r % 20 - 12) * 8; }
constexpr bool skipped(int k) { return ((K4_SKIP & 1) && (k == OPF || k == OCHK || k == OFLG)) || ((K4_SKIP & 4) && k == OX4) || ((K4_SKIP & 8) && k == OW4) || ((K4_SKIP & 16) && k == OST) || ((K4_SKIP & 32) && k == OPS) || ((K4_SKIP & 64) && k == OH) || ((K4_SKIP & 128) && k == OPL) || ((K4_SKIP & 256) && (k == ORW || k == ORR)); }
struct Sched {
  int n; short Q[NO], K[NO], I[NO];
  short first[NG + 4];
  short loc[NO], lb[NK];
  short cvm[NO + 1], clg[NO + 1];
  signed char vm[NG + 3], lg[NG + 3], bar[NG / 2], busy[NG / 2];
  int n0; short q0[NO], k0[NO], i0[NO];
  constexpr void add(int q, int k, int i) { q0[n0] = q; k0[n0] = k; i0[n0] = i; ++n0; }
  constexpr void need(int q, int k, int i) {
    if (skipped(k) || (k == OST && (K4_SKIP & 2))) return;
    int j = loc[lb[k] + i], f = first[q + 2], c = k >= OW3 ? cvm[f] - cvm[j + 1] : clg[f] - clg[j + 1];
    if (Q[j] >= q || K[j] != k || I[j] != i) c = -100;
    if (k >= OW3) { if (c < vm[q + 2]) vm[q + 2] = c; } else { if (c < lg[q + 2]) lg[q + 2] = c; }
  }
  static constexpr int ubar(int j) { return j <= 10 ? 16 * j + 13 : 177 + 16 * (j - 10); }
  static constexpr int uw(int L, int h) {
    if (L <= 8) return 16 * (L - 2) + 2 * h;
    if (L <= 13) return 140 + 8 * (L - 9) + 2 * h;
    return 186 + 16 * (L - 14) + 2 * h;
  }
  static constexpr int uc2(int i) { return i == 0 ? 100 : i == 1 ? 110 : i == 2 ? 118 : i == 3 ? 126 : 134; }
  constexpr int glu(int q, int a, int b, int cap) {
    int used = 0;
    for (int op = VM1; op <= VCV; ++op)
      for (int t = 0; t < (b >= 0 ? 2 : 1); ++t)
        for (int v = 0; v < (op == VCV ? 4 : 8); ++v) {
          int c = (op == VEX || op == VRC) ? 8 : 4;
          if (used && used + c > cap) { ++q; used = 0; }
          add(q, OV, op * 64 + (t ? b : a) * 8 + v); used += c;
        }
    return q + 1;
  }
  constexpr Sched() : n(0), Q{}, K{}, I{}, first{}, loc{}, lb{}, cvm{}, clg{}, vm{}, lg{}, bar{}, busy{}, n0(0), q0{}, k0{}, i0{} {
    int ub[64] = {}, nb = 0, stq[9] = {}, fb[5] = {K4_FB}, pq[6] = {K4_PQ};
    for (int j = 0; j < 19; ++j) ub[nb++] = ubar(j);
    for (int u = 5; u < UD - 4; u += K4_BS) ub[nb++] = U4 + u;
    ub[nb++] = U4 + UD + 3;
    ub[nb++] = U4 + UD + 19;
    for (int j = 0; j < nb; ++j) bar[ub[j]] = 1;
    for (int i = 0; i < G4; ++i) add(i < 6 ? -1 : i - 6, OA3, i);
    for (int u = 0; u < UE; ++u) { if (!(u & 3) && (!u || w4seg(u >> 2) != w4seg((u >> 2) - 1))) add(G4 + 2 * (u - 3), OF4, u >> 2); add(G4 + 2 * (u - 3), OA4, u); }
    int q = glu(2 * U1 + 2, 0, 1, 8);
    stq[0] = (q + 1) & ~1; stq[1] = stq[0] + 4;
    q = glu(2 * U2 + 2, 2, 3, 4);
    stq[2] = (q + 1) & ~1; stq[3] = stq[2] + 16;
    q = glu(q, 4, -1, 4);
    for (int v = 0; v < 2; ++v) add(q + 1 + v, OV, VPL * 64 + 4 * 8 + v);
    stq[4] = (q + 5) & ~1;
    q = glu(2 * U3 + 8, 0, 1, 4);
    stq[5] = (q + 1) & ~1; stq[6] = stq[5] + 16;
    q = glu(G4 - 1, 2, 3, K4_CAP);
    stq[7] = q; stq[8] = q + 2;
    int qadd[7] = {}, qps[4] = {};
    for (int s = 0; s < 7; ++s) add(G4 + 2 * (UD - 7 + s) + 4, ORW, s);
    q = G4 + 2 * (UD + 3);
    for (int s = 0; s < 7; ++s) add(q + s, ORR, s);
    q += 4;
    for (int s = 0; s < 7; ++s) {
      qadd[s] = q;
      for (int i = 0; i < 4; ++i) add(q + (K4_AG == 3 ? i >> 1 : K4_AG == 2 ? i / 3 : 0), OADD, 4 * s + i);
      for (int i = 0; i < 2; ++i) add(q + K4_AG - 1, OCVT, 2 * s + i);
      q += K4_AG;
      if ((s & 1) || s == 6) qps[s >> 1] = q;
    }
    for (int s = 0; s < 5; ++s) add(2 * fb[s], OFLG, s);
    for (int s = 0; s < 6; ++s) add(2 * (pq[s] + K4_PD), OCHK, s);
    for (int i = 0; i < 2; ++i) { add(-2, OS3, i); add(-2, OW3, i); }
    { int pg[15] = {K3_PG};
      for (int k = 0; k < 4; ++k) add(pg[k], OX3, k);
      for (int i = 2; i < 4; ++i) if (pg[7 + i] <= pg[4]) { add(pg[7 + i], OS3, i); add(pg[7 + i], OW3, i); }
      for (int i = 0; i < 5; ++i) add(pg[4 + i], OSQ, i);
      for (int i = 2; i < 4; ++i) if (pg[7 + i] > pg[4]) { add(pg[7 + i], OS3, i); add(pg[7 + i], OW3, i); }
      for (int k = 4; k < 8; ++k) add(pg[7 + k], OX3, k); }
    for (int i = 4; i < 36; ++i) { int u = uw(i >> 1, i & 1); busy[u] = 1; add(2 * u, OS3, i); add(2 * u + 1, OW3, i); }
    for (int i = 0; i < 5; ++i) { int u = uc2(i); busy[u] = 1; add(2 * u, OSC, i); add(2 * u + 1, OC2, i); }
    if (K3_TU >= 0) for (int i = 0; i < K3_TN; ++i) { int u = K3_TU + i * K3_TS; while (busy[u]) ++u; busy[u] = 1; add(2 * u + 1, OT3, i); }
    for (int r = 0, u = 0; r < NR; ++r) {
      int f = r < 6 ? U2 - 1 : r < 20 ? U2 + 15 + 16 * (r / 2 - 1) : U4 + 4 * (r - 20) + 3, j = 0;
      while (ub[j] < f) ++j;
      u += r < 6 ? 8 : K4_WS;
      if (u < ub[j]) u = ub[j];
      while (busy[u]) ++u;
      busy[u] = 1;
      add(2 * u, OS4, r); add(2 * u + 1, OW4, r);
    }
    for (int i = 8; i < 36; ++i) add(2 * (16 * ((i >> 2) - 2) + 5 + 2 * (i & 3)), OX3, i);
    for (int t = 0; t < 9; ++t) add(stq[t], OST, t);
    for (int s = 0; s < 6; ++s) add(2 * pq[s], OPF, s);
    for (int i = 0, g = 0; i < 36; ++i) {
      int s = i < 8 ? 0 : i < 16 ? 1 : i < 20 ? 2 : i < 28 ? 3 : 4, a = 2 * (pq[s] + K4_PD), b = 2 * (U3 + 4 * (i >> 1) + 4);
      g += K4_XS;
      if (g < a) g = a;
      if (g < b) g = b;
      add(g, OX4, i);
    }
    for (int i = 0; i < 3; ++i) add(G4 + 2 * K4_UH + 2 * i, OH, i);
    for (int i = 0; i < 4; ++i) add(qps[i], OPS, i);
    if (UPL >= 0) for (int i = 0; i < 6; ++i) add(G4 + 2 * UPL + K4_PS * i, OPL, i);
    if (K4_UP2 >= 0) for (int i = 0; i < 6; ++i) add(G4 + 2 * K4_UP2 + i, OPL, 6 + i);
    { short cn[NG + 4] = {}, ck[NK] = {};
      for (int j = 0; j < n0; ++j) { ++cn[q0[j] + 2]; ++ck[k0[j]]; }
      int acc = 0;
      for (int k = 0; k < NK; ++k) { lb[k] = acc; acc += ck[k]; }
      acc = 0;
      for (int g = 0; g < NG + 4; ++g) { first[g] = acc; acc += cn[g]; cn[g] = first[g]; }
      for (int j = 0; j < n0; ++j) { int p = cn[q0[j] + 2]++; Q[p] = q0[j]; K[p] = k0[j]; I[p] = i0[j]; if (k0[j] <= ORR || k0[j] >= OW3) loc[lb[k0[j]] + i0[j]] = p; } }
    n = n0;
    for (int j = 0; j < n; ++j) { cvm[j + 1] = cvm[j] + (K[j] >= OW3 && !skipped(K[j])); clg[j + 1] = clg[j] + (K[j] <= ORR && !skipped(K[j])) + (EXPD == 1 && K[j] == OA3 && I[j] < 2 * U1) - (EXPD == 2 && K[j] == OA3 && I[j] < 2 * U1 && !(I[j] & 1)); }
    for (int g = 0; g < NG + 3; ++g) { vm[g] = 63; lg[g] = 15; }
    need(-1, OW3, 1);
    for (int u = 0; u < U4; ++u) need(2 * u, OA3, 2 * u + 1);
    for (int k = 0; k < 18; ++k) need(16 * k, OX3, 2 * k + 1);
    for (int j = 0; j < 19; ++j) { if (j == 8 || j == 9) need(2 * ub[j], OC2, j == 8 ? 1 : 3); else need(2 * ub[j], OW3, j < 8 ? 2 * j + 3 : 2 * j - 1); }
    for (int u = 0; u < UE; ++u) {
      need(G4 + 2 * u, OA4, u);
      if (slot_of(u) == 0) for (int i = 0; i < 2; ++i) need(G4 + 2 * u + i, OX4, 2 * io_of(u) + i);
    }
    for (int j = 18; j < nb; ++j) { int r = j + 1 < nb ? (ub[j + 1] - U4 + 2) / 4 : NR - 1; need(2 * ub[j], OW4, r); }
    for (int s = 0; s < 5; ++s) { need(2 * fb[s], OST, s < 2 ? 2 * s + 1 : 2 * s); need(2 * (pq[s] + K4_PD), OPF, s); if (!bar[fb[s]] || (s && pq[s] < pq[s - 1] + K4_PD)) vm[0] = -1; }
    need(2 * (pq[5] + K4_PD), OPF, 5);
    if (pq[5] < pq[4] + K4_PD || 2 * (pq[5] + K4_PD) >= qps[0] || 2 * (K4_FI + 3 * K4_FS) >= stq[0]) vm[0] = -1;
    need(G4 + 2 * (UD + 3), ORW, 6);
    for (int i = 0, e = K4_UP2 >= 0 ? 12 : UPL >= 0 ? 6 : 0; i < e; ++i) if (cvm[n] - cvm[loc[lb[OPL] + i]] != e - i) vm[0] = -1;
    for (int s = 0; s < 7; ++s) need(qadd[s], ORR, s);
    if (stq[1] >= 2 * U2 || stq[4] >= 2 * U3 + 8 || stq[6] >= G4 - 1 || !bar[U4 + 117] || UD + 19 >= UE) vm[0] = -1;
  }
  constexpr bool ok() const { for (int g = 0; g < NG + 3; ++g) if (vm[g] < 0 || lg[g] < 0) return false; return Q[n - 1] < NG && n <= NO; }
};
constexpr Sched SC{};
static_assert(SC.ok(), "schedule");
}
DI void dma_tc(const u32x4& rs, unsigned voff, unsigned so, unsigned lds) {
  asm volatile("s_mov_b32 m0, %3\n\ts_nop 0\n\tbuffer_load_dword %0, %1, %2 offen lds" ::"v"(voff), "s"(rs), "s"(so), "s"(lds) : "memory");
}
template <int OFF> DI void flag_set(void* fb, unsigned z, int w) {
  asm volatile("s_cmp_lg_u32 %3, 0\n\ts_cbranch_scc1 1f\n\ts_mov_b64 exec, 1\n\tglobal_store_dword %0, %0, %1 offset:%2 sc1\n\ts_mov_b64 exec, -1\n1:" ::"v"(z), "s"(fb), "i"(OFF), "s"(w) : "memory", "scc");
}
template <int OFF> DI void flag_ld(unsigned& f, const void* fb, unsigned l16) {
  asm volatile("s_mov_b64 exec, 15\n\tglobal_load_dword %0, %1, %2 offset:%3 sc1\n\ts_mov_b64 exec, -1" : "=v"(f) : "v"(l16), "s"(fb), "i"(OFF) : "memory");
}
template <int OFF> DI void flag_chk(unsigned& f, const void* fb, unsigned l16) {
  unsigned c;
  asm volatile("v_cmp_ne_u32 vcc, 0, %0\n\ts_and_b32 %1, vcc_lo, 15\n\ts_cbranch_scc0 1f\n\ts_mov_b32 %1, 0x40000\n"
               "0:\n\ts_sleep 1\n\tglobal_load_dword %0, %2, %3 offset:%4 sc1\n\ts_waitcnt vmcnt(0)\n\tv_cmp_ne_u32 vcc, 0, %0\n\ts_and_b32 vcc_lo, vcc_lo, 15\n\ts_cbranch_scc0 1f\n\t"
               "s_sub_u32 %1, %1, 1\n\ts_cbranch_scc0 0b\n1:"
               : "+v"(f), "=&s"(c) : "v"(l16), "s"(fb), "i"(OFF) : "memory", "vcc", "scc");
}

#define PST(i)
__global__ __launch_bounds__(512, 2) void k3_mlp(const u16* Wg, const u16* Wu, char* ws, u16* out) {
  using namespace k3;
  __shared__ __attribute__((aligned(1024))) char smem[163840];
  STAMP_BEGIN();
  const unsigned sb = (unsigned)(size_t)smem;
  const int w = __builtin_amdgcn_readfirstlane(threadIdx.x >> 6);
  const int lane = threadIdx.x & 63;
  const int b = blockIdx.x, xcd = b & 7, bi = b >> 3;
  const int tt = (xcd >> 2) * 4 + (bi >> 3);
  const int fg = (xcd & 3) * 8 + (bi & 7);
  const int G = fg >> 2, j = fg & 3;
  const int tb = tt * 16 + 2 * w;
  const int gu = w >> 2;
  unsigned wlo, w2lo, wdst = sb + gu * 8192 + (w & 3) * 2048, wdst2, c2dst, e0[7];
  int f0 = 3 - G, f1 = 2 * j, f2 = j == 1 ? 8 : 2 * j, f3 = 2 * j + 1;
  { int row = lane >> 3, pos = (lane >> 1) & 3; int tile = pos ^ ((row >> 1) & 3);
    wlo = (row + 16 * (w & 3)) * (F_ * 2) + tile * 32 + (lane & 1) * 16 + G * 9 * 128;
    for (int k = 0; k < 2; ++k) e0[k] = (unsigned)WS_HC + (tb + k) * 18432 + lane * 16;
    for (int i = 0; i < 5; ++i) e0[2 + i] = (unsigned)WS_SSQ + tb * 64 + i * 2 * 8192 + (lane & 31) * 4 + (lane >> 5) * 8192;
    for (int i = 0; i < 7; ++i) opq_v(e0[i]);
    opq_v(wlo); opq_s(wdst); opq_s(f0); opq_s(f1); opq_s(f2); opq_s(f3); }
  opq_s(Wg); opq_s(Wu); opq_s(ws);
  PST(2);
  const u32x4 rsW = mkrsrc(gu ? Wu : Wg, D_ * F_ * 2);
  { const int c = (f0 - (int)((size_t)Wg >> 7)) & 7; wlo += (NOPERM ? f1 : c == f3 ? f3 : c ? f1 : f2) * 128; }
  u32x4 X[36];
  unsigned sq[5];
  const char* xb[2][3];
  char* ab[2]; char* ab2;
  const unsigned l16 = lane * 16;
  unsigned so;
  float sdv[2], nav[2];
  f32x4 acc[5][4];
  unsigned ov[5][4];
  unsigned ra[4];
  const int rrow = (lane >> 4) * 4 + ((lane & 15) >> 2);
  u32x2 A[4][2];
  const int q = b & 3, cg = bi & 3, m = w >> 1, kh = w & 1;
  const int tb4 = (((b >> 2) & 1) * 8 + (bi >> 2)) * 8 + w;
  char* wq = ws; char* oq = (char*)out;
  u32x4 rsD;
  unsigned ddst, rds, rd, rwa, rra, l8, l8m, pf;
  const char* yb[2][3]; char* fs; const char* fp; const char* fq;
  char* psb[4]; const char* plb[2]; const char* hb[3]; char* ob0; char* ob1;
  u32x4 Y[36], B[4], P[4], R[3], sel0, sel1, selb;
  u32x2 Ps[4], Rs[3];
  f32x4 dc[9][2], rr[7];
  unsigned dv[7][2], oo0, oo1, mh;
  int tj;
  auto op = [&](auto jc) AINL {
    constexpr int jj = decltype(jc)::v, k = SC.K[jj], i = SC.I[jj];
    if constexpr (skipped(k)) { if constexpr (k == OX4) undef(Y[i]); if constexpr (k == ORR) undef(rr[i]); if constexpr (k == OH) { undef(P[3]); undef(Ps[3]); undef(mh); } if constexpr (k == OPL) { undef(P[i % 3]); undef(Ps[i % 3]); } }
    else if constexpr (k == OA3) {
      constexpr int u = i >> 1, h = i & 1;
      if constexpr (u < U1) {
        constexpr int t = u & 3, g = u >> 4, kk = (u >> 2) & 3, slot = g % NSLOT;
        if constexpr (EXPD == 1) trd<(slot & 3) * 16384 + (kk & 1) * 8192 + (kk >> 1) * 4096 + (h ^ 1) * 2048>(A[u & 3][h], ra[t]);
        if constexpr (EXPD == 2 && h == 0) { if constexpr (u < 4) undef(A[u & 3][h]); } else
        trd<(slot & 3) * 16384 + (kk & 1) * 8192 + (kk >> 1) * 4096 + h * 2048>(A[u & 3][h], ra[t]);
      } else if constexpr (u < U2) {
        constexpr int n = u - U1;
        trd<(n & 1) * 18432 + (n >> 1) * 1024 + h * 512>(A[u & 3][h], ra[0]);
      } else {
        constexpr int n = u - U2, slot = (9 + (n >> 4)) % NSLOT;
        trd<(slot & 3) * 16384 + ((n >> 3) & 1) * 8192 + ((n >> 1) & 1) * 4096 + ((n >> 2) & 1) * 2048 + h * 1024>(A[u & 3][h], ra[2 + (n & 1)]);
      }
    } else if constexpr (k == OV) {
      constexpr int o = i >> 6, sl = (i >> 3) & 7, v = i & 7, tk = v >> 2;
      if constexpr (o < VCV) {
        float t = acc[sl][tk][v & 3];
        if constexpr (o == VM1) a_mul1(t, nav[tk]);
        else if constexpr (o == VEX) a_exp(t);
        else if constexpr (o == VFM) a_fma11(t, sdv[tk]);
        else if constexpr (o == VRC) a_rcp(t);
        else a_mul1(t, acc[sl][2 + tk][v & 3]);
        acc[sl][tk][v & 3] = t;
      } else if constexpr (o == VCV) a_cvt(ov[sl][v], acc[sl][v >> 1][2 * (v & 1)], acc[sl][v >> 1][2 * (v & 1) + 1]);
      else a_pl16(ov[sl][v], ov[sl][2 + v]);
    } else if constexpr (k == OS3) {
      if constexpr (i < 18) dma_set<((i >> 1) * 64 + (i & 1) * 8) * (F_ * 2), ((i >> 1) % NSLOT) * 16384 + (i & 1) * 1024>(so, wdst);
      else { constexpr int hs = i - 18; dma_set<(hs % 9) * 64 * (F_ * 2) + (hs / 9) * 64, ((i >> 1) % NSLOT) * 16384 + (i & 1) * 8192>(so, wdst2); }
    } else if constexpr (k == OSC) dma_set<i * 128 * (F_ * 2), i * 4096>(so, c2dst);
    else if constexpr (k == OW3) dma_go(rsW, wlo, so);
    else if constexpr (k == OC2) dma_go(rsW, w2lo, so);
    else if constexpr (k == OX3 && i < 8) gload16<(i >> 1) * 1024>(X[i], ws, e0[i & 1]);
    else if constexpr (k == OX3) gload16<((i >> 1) & 7) * 1024 - 4096>(X[i], xb[i & 1][i >> 4], l16);
    else if constexpr (k == OSQ) gload4<0>(sq[i], ws, e0[2 + i]);
    else if constexpr (k == OST) {
      if constexpr (i == 4) {
        unsigned ln = l16; opq_v(ln);
        const unsigned off = (ln & 256) * 576 + (ln >> 9) * 256 + (ln & 240);
        gstore16_wt<0>(ab2, off, u32x4{ov[4][0], ov[4][1], ov[4][2], ov[4][3]});
      } else {
        constexpr int i2 = i > 4 ? i - 1 : i, pr = i2 >> 1, tk = i2 & 1, a = (pr & 1) * 2;
        gstore16_wt<pr * 1024>(ab[tk], l16, u32x4{ov[a][2 * tk], ov[a][2 * tk + 1], ov[a + 1][2 * tk], ov[a + 1][2 * tk + 1]});
      }
    } else if constexpr (k == OT3) {
      constexpr int n = 36 / K3_TN;
      unsigned o = min(lane, n - 1) * (16 * F_ * 2); opq_v(o);
      dma_tc(rsW, o, (i * n * 16 + (bi >> 3) * 4 + (w & 3)) * (F_ * 2) + fperm(Wg, G, 2 * j + 1) * 128, sb + 159 * 1024);
    } else if constexpr (k == OFLG) flag_set<i * 128>(fs, l16, w);
    else if constexpr (k == OPF) {
      if constexpr (i < 5) flag_ld<i * 128>(pf, fp, l16);
      else { l8 = l16 << 6; opq_v(l8); flag_ld<0>(pf, fq, l8); }
    } else if constexpr (k == OCHK) {
      if constexpr (i < 5) flag_chk<i * 128>(pf, fp, l16); else flag_chk<0>(pf, fq, l8);
    }
    else if constexpr (k == OF4) {
      if constexpr (i == 0) { rd = rds + l16; opq_v(rd); } else vadd_opq<(w4seg(i) - w4seg(i - 1)) * 1024>(rd);
    } else if constexpr (k == OA4) rd128<w4off(i >> 2) * 1024 + (i & 3) * 2048>(B[i & 3], rd);
    else if constexpr (k == ORW) wr128<i * 1024>(rwa, dc[i][1]);
    else if constexpr (k == ORR) rd128f<i * 1024>(rr[i], rra);
    else if constexpr (k == OADD) { float t = dc[i >> 2][0][i & 3]; a_add(t, rr[i >> 2][i & 3]); dc[i >> 2][0][i & 3] = t; }
    else if constexpr (k == OCVT) a_cvt(dv[i >> 1][i & 1], dc[i >> 1][0][2 * (i & 1)], dc[i >> 1][0][2 * (i & 1) + 1]);
    else if constexpr (k == OS4) dma_set<i * 8192, (w4seg(i) + w4off(i)) * 1024>(so, ddst);
    else if constexpr (k == OW4) dma_go(rsD, l16, so);
    else if constexpr (k == OX4) { constexpr int ks = ks_io(i >> 1); gload16_c<(ks & 7) * 1024 - 4096>(Y[i], yb[i & 1][ks >> 3], l16); }
    else if constexpr (k == OH) {
      if constexpr (i == 0) gload16<0>(P[3], hb[0], l16);
      else if constexpr (i == 1) gload8<0>(Ps[3], hb[1], l16);
      else { mh = l16; opq_v(mh); gload4<0>(mh, hb[2], (mh >> 2) & 60); }
    }
    else if constexpr (k == OPS) {
      if constexpr (i < 3) gstore16_wt<0>(psb[i], l16, u32x4{dv[2 * i][0], dv[2 * i][1], dv[2 * i + 1][0], dv[2 * i + 1][1]});
      else gstore8_wt<0>(psb[3], l8, u32x2{dv[6][0], dv[6][1]});
    } else if constexpr (k == OPL) {
      if constexpr (i < 3) gload16_c<i * 1024>(P[i], plb[0], l16); else if constexpr (i < 6) gload8_c<(i - 3) * 1024>(Ps[i - 3], plb[1], l8m);
      else if constexpr (i < 9) gload16_c<(i - 6) * 1024>(R[i - 6], plb[0], l16); else gload8_c<(i - 9) * 1024>(Rs[i - 9], plb[1], l8m);
    }
  };
  sfor<0, SC.first[1]>(op);
  PST(3);
  const int b_ = b, w_ = w;
  int tbf;
  unsigned rws, rrs, mq, w3s, c2s;
  { int b = b_, w = w_; unsigned ln = l16; opq_s(b); opq_s(w); opq_s(wq); opq_s(oq); opq_v(ln);
    const int xcd = b & 7, bi = b >> 3, fg = (xcd & 3) * 8 + (bi & 7), G = fg >> 2, j = fg & 3, tb = ((xcd >> 2) * 4 + (bi >> 3)) * 16 + 2 * w, gu = w >> 2;
    const int q = b & 3, m = w >> 1, kh = w & 1, tb4 = (((b >> 2) & 1) * 8 + (bi >> 2)) * 8 + w;
    for (int k = 0; k < 2; ++k) for (int i = 0; i < 3; ++i) { xb[k][i] = wq + ((unsigned)WS_HC + (tb + k) * 18432 + 4096 + i * 8192); opq_s(xb[k][i]); }
    for (int k = 0; k < 2; ++k) { ab[k] = wq + ((unsigned)WS_ACT + ((tb + k) * 144 + G * 18 + 4 * j) * 1024); opq_s(ab[k]); }
    ab2 = wq + ((unsigned)WS_ACT + (tb * 144 + G * 18 + 16 + (j >> 1)) * 1024 + (j & 1) * 512); opq_s(ab2);
    w2lo = ((ln >> 5) + 32 * (w & 3)) * (F_ * 2) + (ln & 16) + fperm(Wg, G, 8) * 128 + j * 32; opq_v(w2lo);
    wdst2 = sb + gu * 4096 + (w & 3) * 1024; c2dst = sb + C2OFF + gu * 18432 + (w & 3) * 1024; opq_s(wdst2); opq_s(c2dst);
    ddst = sb + w * 1024; opq_s(ddst);
    char* f = wq + WS_FG + (xcd + (bi >> 3) * 8) * 1024; fs = f + (bi & 7) * 16; fp = f + kh * 64; fq = fs - q * 1024; opq_s(fs); opq_s(fp); opq_s(fq);
    tj = tb4 - min(((tb4 * 147) >> 10) * 7, 121);
    tbf = 16 * tb4 - ((0x54 >> tj) & 1) * 15; opq_s(tbf);
    rws = sb + (w < 5 ? 120 : 24) * 1024 + w * 8192; rrs = sb + ((w ^ 1) < 5 ? 120 : 24) * 1024 + (w ^ 1) * 8192; mq = (unsigned)((m ^ q) - 1 >> 31);
    w3s = 16 * (w & 3) * (F_ * 2) + fperm(Wg, G, 2 * j + 1) * 128; c2s = (w & 3) >= 2 ? 64 * (F_ * 2) : 0;
    rds = sb + w4seg(0) * 1024 + kh * 1024;
    opq_s(rws); opq_s(rrs); opq_s(mq); opq_s(tj); opq_s(w3s); opq_s(c2s); opq_s(rds); }
  for (int t = 0; t < 4; ++t) ra[t] = sb + rrow * 128 + ((t ^ ((rrow >> 1) & 3)) * 32) + (lane & 3) * 8;
  auto fixA = [&](auto uc) AINL {
    constexpr int u = decltype(uc)::v;
    if constexpr (u < U1 && u >= 16 && (u & 15) < 4) {
      constexpr int g = u >> 4, a = (g % NSLOT) >> 2, p = ((g - 1) % NSLOT) >> 2;
      if constexpr (a != p) vadd_opq<(a - p) * 65536>(ra[u & 3]);
    }
    if constexpr (u == U1) { unsigned ln = l16; opq_v(ln); ra[0] = sb + C2OFF + (ln >> 1); opq_v(ra[0]); }
    if constexpr (u == U2) { unsigned ln = l16; opq_v(ln); ra[2] = sb + (ln & ~63u) + ((ln >> 3) & 32) + ((ln >> 1) & 24); opq_v(ra[2]); }
    if constexpr (u == U2 + 1) { ra[3] = ra[2] ^ 32; opq_v(ra[3]); }
    if constexpr (u >= U2 + 16 && u < U4 && ((u - U2) & 15) < 2) {
      constexpr int g = 9 + ((u - U2) >> 4), a = (g % NSLOT) >> 2, p = ((g - 1) % NSLOT) >> 2;
      if constexpr (a != p) vadd_opq<(a - p) * 65536>(ra[2 + (u & 1)]);
    }
  };
  PST(4);
  wait_vm<SC.vm[1]>();
  PST(5);
  abar();
  sfor<SC.first[1], SC.first[2]>(op);
  sfor<0, U4>([&](auto uc) AINL {
    constexpr int u = decltype(uc)::v;
    if constexpr (stp(u) > 0) { STAMP_MID(stp(u)); }
    if constexpr (u == Sched::uc2(4)) {
      w2lo -= c2s; c2dst -= (w & 2) * 1024;
      opq_v(w2lo); opq_s(c2dst);
    }
    if constexpr (u == 113) {
      int b = b_, w = w_; opq_s(b); opq_s(w); opq_s(wq);
      const int q = b & 3, m = w >> 1, kh = w & 1;
      char* pb = wq + WS_PART + (size_t)(b & ~3) * PART_BLK;
      for (int t = 0; t < 3; ++t) { psb[t] = pb + (size_t)((q + 1 + t) & 3) * PART_BLK + w * 3072 + t * 1024; opq_s(psb[t]); }
      psb[3] = pb + (size_t)m * PART_BLK + 24576 + ((m - q - 1) & 3) * 1024 + kh * 512; opq_s(psb[3]);
      plb[0] = pb + (size_t)q * PART_BLK + w * 3072; plb[1] = pb + (size_t)q * PART_BLK + 24576 + kh * 512; opq_s(plb[0]); opq_s(plb[1]);
    }
    if constexpr (u == 121) {
      int b = b_, w = w_; opq_s(b); opq_s(w); opq_s(wq);
      const int q = b & 3, cg = (b >> 3) & 3, tb4 = (((b >> 2) & 1) * 8 + (b >> 5)) * 8 + w;
      hb[0] = wq + WS_HC + ((size_t)tb4 * 18 + (cg >> 1) * 9 + (cg & 1) * 5 + q) * 1024;
      hb[1] = wq + WS_HC + ((size_t)tb4 * 18 + (cg >> 1) * 9 + 4) * 1024 + (cg & 1) * 8; hb[2] = wq + WS_SSQ + 9 * 8192 + tb4 * 64; opq_s(hb[0]); opq_s(hb[1]); opq_s(hb[2]);
      char* ob = oq + (size_t)tbf * 1152 + (cg >> 1) * 576;
      ob0 = ob + ((cg & 1) * 5 + q) * 64; ob1 = ob + 256 + (cg & 1) * 32; opq_s(ob0); opq_s(ob1);
    }
    if constexpr (u == 129) {
      int b = b_, w = w_; opq_s(b); opq_s(w); opq_s(wq);
      const int q = b & 3, cg = (b >> 3) & 3, tb4 = (((b >> 2) & 1) * 8 + (b >> 5)) * 8 + w, kh = w & 1;
      rsD = mkrsrc(wq + WS_WDP + (size_t)(q * 4 + cg) * (324 * 1024) + w * 1024, (324 - w) * 1024); opq_s(rsD);
      for (int k = 0; k < 2; ++k) for (int i = 0; i < 3; ++i) { yb[k][i] = wq + WS_ACT + ((size_t)(tb4 ^ k) * 144 + q * 36 + kh * 18) * 1024 + 4096 + i * 8192; opq_s(yb[k][i]); }
    }
    if constexpr (u == Sched::uw(9, 0)) {
      unsigned ln = l16; opq_v(ln);
      wlo = __umul24(ln >> 6, F_ * 2) + (((ln >> 3) & 32 ^ ln) & 48) + w3s; opq_v(wlo);
    }
    wait_vl2<SC.vm[2 * u + 2], SC.lg[2 * u + 2]>();
    if constexpr (SC.bar[u]) abar();
    if constexpr (u == 16) {
      opq_v(sq[0]); opq_v(sq[1]); opq_v(sq[2]); opq_v(sq[3]); opq_v(sq[4]);
      float s = (__uint_as_float(sq[0]) + __uint_as_float(sq[1])) + (__uint_as_float(sq[2]) + __uint_as_float(sq[3])) + (lane < 32 ? __uint_as_float(sq[4]) : 0.f);
      auto sw = __builtin_amdgcn_permlane32_swap(__float_as_uint(s), __float_as_uint(s), false, false);
      s = __uint_as_float(sw[0]) + __uint_as_float(sw[1]);
      float var = rbf(s * (1.f / D_));
      float sd = rbf(__builtin_sqrtf(rbf(var + 1e-5f)));
      auto s2 = __builtin_amdgcn_permlane16_swap(__float_as_uint(sd), __float_as_uint(sd), false, false);
      sdv[0] = __uint_as_float(s2[0]); sdv[1] = __uint_as_float(s2[1]);
      nav[0] = -1.44269504088896f * __builtin_amdgcn_rcpf(sdv[0]); nav[1] = -1.44269504088896f * __builtin_amdgcn_rcpf(sdv[1]);
      opq_v(sdv[0]); opq_v(sdv[1]); opq_v(nav[0]); opq_v(nav[1]);
    }
    constexpr int sl = u < U1 ? (u & 3) : u < U2 ? 4 : u < U3 ? (u & 1) : 2 + (u & 1);
    constexpr int m2 = u < U1 ? ((u >> 2) & 1) * 2 : u < U2 ? (u & 1) * 2 : (u & 2);
    constexpr int ks = u < U1 ? u >> 3 : u < U2 ? (u - U1) >> 1 : u < U3 ? (u - U2) >> 2 : (u - U3) >> 2;
    const u32x4 av = cat4(A[u & 3][0], A[u & 3][1]);
    if constexpr (ks == 0) mf0(acc[sl][m2], av, X[0]); else mf(acc[sl][m2], av, X[2 * ks]);
    sfor<SC.first[2 * u + 2], SC.first[2 * u + 3]>(op);
    wait_vl2<SC.vm[2 * u + 3], SC.lg[2 * u + 3]>();
    if constexpr (ks == 0) mf0(acc[sl][m2 + 1], av, X[1]); else mf(acc[sl][m2 + 1], av, X[2 * ks + 1]);
    sfor<SC.first[2 * u + 3], SC.first[2 * u + 4]>(op);
    fixA(IC<u + 4>{});
  });
  sfor<0, 2 * UE>([&](auto qc) AINL {
    constexpr int g = decltype(qc)::v, u = g >> 1, i = g & 1, sl = slot_of(u), io = io_of(u);
    if constexpr (i == 0 && stp(U4 + u) > 0) { STAMP_MID(stp(U4 + u)); }
    wait_vl2<SC.vm[G4 + g + 2], SC.lg[G4 + g + 2]>();
    if constexpr (i == 0 && SC.bar[U4 + u]) abar();
    if constexpr (io == 0) mf0(dc[sl][i], B[u & 3], Y[i]); else mf(dc[sl][i], B[u & 3], Y[2 * io + i]);
    sfor<SC.first[G4 + g + 2], SC.first[G4 + g + 3]>(op);
    if constexpr (g == 217) { rwa = rws + l16; opq_v(rwa); }
    if constexpr (g == 219) { rra = rrs + l16; opq_v(rra); }
    if constexpr (g == 221) { l8 = l16 >> 1; opq_v(l8); }
    if constexpr (g == 223) { l8m = l8 & mq; opq_v(l8m); }
    if constexpr (g == 2 * UE - 21) {
      opq_v(l8);
      const unsigned one = ((l8 >> 5) & 3) == (l8 >> 7) ? 0x3f80u << ((l8 & 8) * 2) : 0u;
      const unsigned d0 = (l8 & 16) ? 0u : one, d1 = (l8 & 16) ? one : 0u;
      selb = u32x4{d0, d1, d0, d1}; opq_v(selb);
    }
    if constexpr (g == 2 * UE - 17) { sel0 = u32x4{selb[0], selb[1], 0u, 0u}; opq_v(sel0); }
    if constexpr (g == 2 * UE - 15) { sel1 = u32x4{0u, 0u, selb[0], selb[1]}; opq_v(sel1); }
    if constexpr (g == 2 * UE - 13) { opq_v(l8); oo1 = tb_tok(tj, (l8 >> 3) & 15) * 1152; oo0 = oo1 + ((l8 >> 7) & 1) * 32 + (l8 >> 8) * 16; oo1 += (l8 >> 7) * 8; opq_v(oo0); opq_v(oo1); }
  });
  STAMP_MID(6);
  auto look = [&](u32x4* a, u32x2* b) AINL { sfor<0, 3>([&](auto ic) AINL { constexpr int i = decltype(ic)::v; gload16_c<i * 1024>(a[i], plb[0], l16); });
    sfor<0, 3>([&](auto ic) AINL { constexpr int i = decltype(ic)::v; gload8_c<i * 1024>(b[i], plb[1], l8m); }); };
  static_assert(UPL >= 0 || K4_UP2 <= -3);
  if constexpr (UPL == -1) look(P, Ps);
  if constexpr (K4_UP2 == -1) look(R, Rs);
  asm volatile("s_nop 7\n\ts_nop 3");
  wr128<1024>(rwa, dc[7][1]); wr128<2048>(rwa, dc[8][1]);
  if constexpr (K4_UP2 == -2) look(R, Rs);
  wait_vl2<63, 0>();
  abar();
  rd128f<1024>(rr[0], rra); rd128f<2048>(rr[1], rra);
  if constexpr (UPL == -2) look(P, Ps);
  if constexpr (K4_ONE) { for (int i = 0; i < 3; ++i) { undef(R[i]); undef(Rs[i]); } } else
  if constexpr (K4_UP2 <= -3) { __builtin_amdgcn_s_sleep(-3 - (K4_UP2)); look(R, Rs); }
  asm volatile("s_waitcnt vmcnt(6) lgkmcnt(0)" : "+v"(P[3]), "+v"(Ps[3]), "+v"(rr[0]), "+v"(rr[1]), "+v"(mh)::"memory");
  u64x2 T[5];
  T[4][1] = __builtin_bit_cast(u64, Ps[3]);
  auto missing = [&](const u32x4* a, const u32x2* b) AINL {
    for (int i = 0; i < 9; ++i) { u64 t; asm volatile("v_mov_b64 %0, %1" : "=v"(t) : "v"(i < 6 ? __builtin_bit_cast(u64x2, a[i >> 1])[i & 1] : __builtin_bit_cast(u64, b[i - 6]))); T[i >> 1][i & 1] = t; }
    const u32x4 t0 = __builtin_bit_cast(u32x4, T[0]), t1 = __builtin_bit_cast(u32x4, T[1]), t2 = __builtin_bit_cast(u32x4, T[2]), t3 = __builtin_bit_cast(u32x4, T[3]), t4 = __builtin_bit_cast(u32x4, T[4]);
    unsigned mx = umax3(umax3(t0[0], t0[1], t0[2]), umax3(t0[3], t1[0], t1[1]), umax3(t1[2], t1[3], t2[0]));
    mx = umax3(mx, t2[1], __builtin_elementwise_max(t2[2], t2[3]));
    if (m == q) mx = umax3(mx, umax3(t3[0], t3[1], t3[2]), umax3(t3[3], t4[0], t4[1]));
    return __builtin_amdgcn_ballot_w64(mx == 0xffffffffu) != 0;
  };
  auto again = [&](const u32x4* a, const u32x2* b) AINL {
    if constexpr (K4_SL > 0) __builtin_amdgcn_s_sleep(K4_SL);
    asm volatile("global_load_dwordx4 %0, %6, %8 sc1\n\tglobal_load_dwordx4 %1, %6, %8 offset:1024 sc1\n\tglobal_load_dwordx4 %2, %6, %8 offset:2048 sc1\n\t"
                 "global_load_dwordx2 %3, %7, %9 sc1\n\tglobal_load_dwordx2 %4, %7, %9 offset:1024 sc1\n\tglobal_load_dwordx2 %5, %7, %9 offset:2048 sc1\n\ts_waitcnt vmcnt(6)"
                 ::"v"(a[0]), "v"(a[1]), "v"(a[2]), "v"(b[0]), "v"(b[1]), "v"(b[2]), "v"(l16), "v"(l8m), "s"(plb[0]), "s"(plb[1]) : "memory"); };
  mf(dc[7][0], sel0, P[3]); mf(dc[8][0], sel1, P[3]);
  rr[0] += __uint_as_float(mh); rr[1] += __uint_as_float(mh);
  if constexpr (K4_ONE) asm volatile("s_waitcnt vmcnt(0)" : "+v"(rr[0]), "+v"(rr[1])::"memory");
#pragma unroll 1
  for (int spin = 0; missing(P, Ps) && spin < (1 << 16); ++spin) {
    again(P, Ps);
    if constexpr (K4_ONE) asm volatile("s_waitcnt vmcnt(0)" ::: "memory");
    else {
      if (!missing(R, Rs)) break;
      again(R, Rs);
    }
  }
  asm volatile("s_nop 1" : "+v"(T[0]), "+v"(T[1]), "+v"(T[2]), "+v"(T[3]), "+v"(T[4]));
  sfor<0, 3>([&](auto ic) AINL { constexpr int i = decltype(ic)::v; mf(dc[7][0], sel0, __builtin_bit_cast(u32x4, T[i])); mf(dc[8][0], sel1, __builtin_bit_cast(u32x4, T[i])); });
  if (m == q) { mf(dc[6][0], selb, __builtin_bit_cast(u32x4, T[3])); mf(dc[6][0], selb, __builtin_bit_cast(u32x4, T[4])); }
  asm volatile("s_nop 7\n\ts_nop 3" : "+v"(dc[6][0]), "+v"(dc[7][0]), "+v"(dc[8][0]), "+v"(rr[0]), "+v"(rr[1])::"memory");
  dc[6][0] += __uint_as_float(mh);
  { const f32x4 a = dc[7][0] + rr[0], c = dc[8][0] + rr[1];
    auto s0 = __builtin_amdgcn_permlane16_swap(pk2(a[0], a[1]), pk2(c[0], c[1]), false, false);
    auto s1 = __builtin_amdgcn_permlane16_swap(pk2(a[2], a[3]), pk2(c[2], c[3]), false, false);
    gstore16_out(ob0, oo0, u32x4{s0[0], s1[0], s0[1], s1[1]});
    if (m == q) gstore8_out(ob1, oo1, u32x2{pk2(dc[6][0][0], dc[6][0][1]), pk2(dc[6][0][2], dc[6][0][3])});
  }
  STAMP_END(ws, 2);
  if constexpr (!K4_ONE) asm volatile("s_waitcnt vmcnt(0)" ::"v"(P[0]), "v"(P[1]), "v"(P[2]), "v"(Ps[0]), "v"(Ps[1]), "v"(Ps[2]), "v"(R[0]), "v"(R[1]), "v"(R[2]), "v"(Rs[0]), "v"(Rs[1]), "v"(Rs[2]) : "memory");
  asm volatile("k_text1:");
}

static constexpr int g_mask = 15;
extern "C" void kernel_launch(void* const* d_in, const int* in_sizes, int n_in, void* d_out, int out_size, void* d_ws, size_t ws_size,
                              hipStream_t stream) {
  const u16* x = (const u16*)d_in[0];
  const u16* wqkv = (const u16*)d_in[1];
  const u16* wo = (const u16*)d_in[2];
  const u16* wg = (const u16*)d_in[4];
  const u16* wu = (const u16*)d_in[5];
  const u16* wd = (const u16*)d_in[6];
  char* ws = (char*)d_ws;
  if (g_mask & 1) k1_attn<<<256, 768, 0, stream>>>(x, wqkv, wo, ws, wg, wu, wd);
  if (g_mask & 4) k3_mlp<<<256, 512, 0, stream>>>(wg, wu, ws, (u16*)d_out);
}
```
